```python
import torch

def _softmax(x, axis=-1):
    return torch.softmax(x, dim=axis)

def _gelu(x):
    return torch.nn.functional.gelu(x, approximate='tanh')

def _silu(x):
    return torch.nn.functional.silu(x)

def _sigmoid(x):
    return torch.sigmoid(x)

def _layernorm(x, axis=-1, eps=1e-05):
    m = x.mean(dim=axis, keepdim=True)
    v = x.var(dim=axis, keepdim=True, unbiased=False)
    return (x - m) / torch.sqrt(v + eps)

def _scatter_add(tgt, idx, src, axis=0):
    return tgt.clone().index_add_(axis, idx, src)

def _segment_sum(src, seg, n):
    out = torch.zeros((n,) + src.shape[1:], dtype=src.dtype)
    return out.index_add_(0, seg, src)

def _pad(x, pw):
    flat = tuple((v for pair in reversed(pw) for v in pair))
    return torch.nn.functional.pad(x, flat)
kernel_name = 'adaLN_cond__memory_aug_recurrent_mem_tr__dense_activation_gelu__prompt'

def setup_inputs():

    _w = lambda a: (lambda t: t.to(torch.bfloat16) if t.is_floating_point() else t)(torch.as_tensor(a))

    def _A_setup():
        B, S, Dm = (1, 2048, 512)
        H, Dh = (32, 128)
        M, seg = (8, 32)
        seg = min(seg, S)
        S = S // seg * seg
        return {'x': _w((lambda *s: torch.randn(*s, dtype=torch.bfloat16) * 0.05)(B, S, Dm)), 'mem0': _w((lambda *s: torch.randn(*s, dtype=torch.bfloat16) * 0.8)(B, M, Dm)), 'Wq': _w((lambda *s: torch.randn(*s, dtype=torch.bfloat16) * 1.6)(Dm, H, Dh) * Dm ** (-0.5)), 'Wk': _w((lambda *s: torch.randn(*s, dtype=torch.bfloat16) * 1.6)(Dm, H, Dh) * Dm ** (-0.5)), 'Wv': _w((lambda *s: torch.randn(*s, dtype=torch.bfloat16) * 0.8)(Dm, H, Dh) * Dm ** (-0.5)), 'Wo': _w((lambda *s: torch.randn(*s, dtype=torch.bfloat16) * 0.8)(H, Dh, Dm) * (H * Dh) ** (-0.5)), 'seg_len': seg}
    _A_in = _A_setup()

    def _B_setup():
        B, S, D, F = (1, 2048, 512, 1376)
        return {'x': _w((lambda *s: torch.randn(*s, dtype=torch.bfloat16) * 0.05)(B, S, D)), 'w1': _w((lambda *s: torch.randn(*s, dtype=torch.bfloat16) * 1.6)(D, F) * D ** (-0.5)), 'b1': _w((lambda *s: torch.randn(*s, dtype=torch.bfloat16) * 1.6)(F) * 0.01), 'w2': _w((lambda *s: torch.randn(*s, dtype=torch.bfloat16) * 0.2)(F, D) * F ** (-0.5)), 'b2': _w((lambda *s: torch.randn(*s, dtype=torch.bfloat16) * 0.2)(D) * 0.01)}
    _B_in = _B_setup()
    return {'A_x': _A_in['x'], 'A_mem0': _A_in['mem0'], 'A_Wq': _A_in['Wq'], 'A_Wk': _A_in['Wk'], 'A_Wv': _A_in['Wv'], 'A_Wo': _A_in['Wo'], 'A_seg_len': _A_in['seg_len'], 'B_x': _B_in['x'], 'B_w1': _B_in['w1'], 'B_b1': _B_in['b1'], 'B_w2': _B_in['w2'], 'B_b2': _B_in['b2'], 'c': _w((lambda *s: torch.randn(*s, dtype=torch.bfloat16) * 0.05)(1, 512) * 8.0), 'w_mod': _w((lambda *s: torch.randn(*s, dtype=torch.bfloat16) * 0.05)(512, 6 * 512) * 512 ** (-0.5) * 8.0)}

def reference(A_x, A_mem0, A_Wq, A_Wk, A_Wv, A_Wo, A_seg_len, B_x, B_w1, B_b1, B_w2, B_b2, c, w_mod):

    def _A_ref(x, mem0, Wq, Wk, Wv, Wo, seg_len):
        B, S, Dm = x.shape
        H, Dh = (Wq.shape[1], Wq.shape[2])
        M = mem0.shape[1]
        scale = Dh ** (-0.5)
        n_seg = S // seg_len
        L = M + seg_len
        idx = torch.arange(L)
        mask = (idx[:, None] < M) | (idx[:, None] >= idx[None, :])
        mem = mem0
        outs = []
        for s in range(n_seg):
            xs = x[:, s * seg_len:(s + 1) * seg_len, :].clone()
            h = torch.concatenate([mem, xs], axis=1)
            q = torch.einsum('btd,dhe->bhte', h, Wq)
            k = torch.einsum('btd,dhe->bhte', h, Wk)
            v = torch.einsum('btd,dhe->bhte', h, Wv)
            scores = torch.einsum('bhqe,bhke->bhqk', q, k) * scale
            scores = torch.where(mask[None, None], scores, -1000000000.0)
            p = _softmax(scores, axis=-1)
            o = torch.einsum('bhqk,bhke->bhqe', p, v)
            o = torch.einsum('bhte,hed->btd', o, Wo)
            mem = o[:, :M, :].clone()
            outs.append(o[:, M:, :])
        return torch.concatenate(outs, axis=1)

    def _B_ref(x, w1, b1, w2, b2):
        h = torch.einsum('bsd,df->bsf', x, w1) + b1
        h = _gelu(h)
        return torch.einsum('bsf,fd->bsd', h, w2) + b2
    x = A_x
    mod = (_silu(c) @ w_mod).reshape(c.shape[0], 6, 1, -1)
    sa, ca, ga, sb, cb, gb = (mod[:, 0].clone(), mod[:, 1].clone(), mod[:, 2].clone(), mod[:, 3].clone(), mod[:, 4].clone(), mod[:, 5].clone())
    h = _layernorm(x, axis=-1) * (1 + ca) + sa
    x = x + ga * _A_ref(h, A_mem0, A_Wq, A_Wk, A_Wv, A_Wo, A_seg_len)
    h = _layernorm(x, axis=-1) * (1 + cb) + sb
    x = x + gb * _B_ref(h, B_w1, B_b1, B_w2, B_b2)
    return x
```

```cpp
#include <hip/hip_runtime.h>
#include <hip/hip_bf16.h>

typedef unsigned u32;
typedef unsigned u32x2 __attribute__((ext_vector_type(2)));
typedef unsigned u32x4 __attribute__((ext_vector_type(4)));
typedef unsigned u32x8 __attribute__((ext_vector_type(8)));
typedef float f32x2 __attribute__((ext_vector_type(2)));
typedef float f32x4 __attribute__((ext_vector_type(4)));
typedef short s16x4 __attribute__((ext_vector_type(4)));
typedef __bf16 bf2 __attribute__((ext_vector_type(2)));
typedef __bf16 bf8 __attribute__((ext_vector_type(8)));
typedef __bf16 bf16v __attribute__((ext_vector_type(16)));
typedef __amdgpu_buffer_rsrc_t rsrc_t;
#define LAS __attribute__((address_space(3)))
#define DEV __device__ __forceinline__
constexpr int SC1 = 16;
constexpr u32 SPIN_CAP = 400000;

constexpr u32 MB = 1u << 20;
constexpr u32 O_MEM = 0;
constexpr u32 O_RING = 1 * MB;
constexpr u32 O_CTRL = 2 * MB;
constexpr u32 O_MOD = 2 * MB + 131072;
constexpr u32 O_H = 4 * MB, O_X1 = 6 * MB, O_H2 = 8 * MB, O_KM = 10 * MB, O_VM = 14 * MB;
constexpr u32 O_Q = 18 * MB, O_K = 34 * MB, O_V = 50 * MB, O_P1 = 66 * MB, O_P2 = 98 * MB, WS_END = 120 * MB;
constexpr u32 F_TMO = 0, F_SIDE = 1, F_H = 2, F_KV = F_H + 64, F_KM = F_KV + 192, F_P1 = F_KM + 64, F_H2 = F_P1 + 64,
              F_P2 = F_H2 + 64, F_END = F_P2 + 64;

struct Params {
  const __bf16 *x, *mem0, *wq, *wk, *wv, *wo, *w1, *b1, *w2, *b2, *c, *wmod;
  __bf16* out;
  char* wsraw;
};

DEV char* align_ws(char* p) { return p + ((0 - (size_t)p) & (2 * MB - 1)); }
DEV rsrc_t mkrsrc(const void* p, u32 n) { return __builtin_amdgcn_make_buffer_rsrc((void*)p, 0, n, 0x00020000); }
DEV u32x4 ld16(rsrc_t r, u32 v, u32 s) { return __builtin_amdgcn_raw_buffer_load_b128(r, v, s, SC1); }
DEV u32x2 ld8(rsrc_t r, u32 v, u32 s) { return __builtin_amdgcn_raw_buffer_load_b64(r, v, s, SC1); }
DEV u32 ld4(rsrc_t r, u32 v, u32 s) { return __builtin_amdgcn_raw_buffer_load_b32(r, v, s, SC1); }
DEV void st16(u32x4 d, rsrc_t r, u32 v, u32 s) { __builtin_amdgcn_raw_buffer_store_b128(d, r, v, s, SC1); }
DEV void st8(u32x2 d, rsrc_t r, u32 v, u32 s) { __builtin_amdgcn_raw_buffer_store_b64(d, r, v, s, SC1); }
DEV void st4(u32 d, rsrc_t r, u32 v, u32 s) { __builtin_amdgcn_raw_buffer_store_b32(d, r, v, s, SC1); }
DEV u32 pk(float a, float b) { f32x2 v = {a, b}; return __builtin_bit_cast(u32, __builtin_convertvector(v, bf2)); }
DEV float lo(u32 v) { return __uint_as_float(v << 16); }
DEV float hi(u32 v) { return __uint_as_float(v & 0xffff0000u); }
DEV float rb(float a) { return lo(pk(a, a)); }
DEV u32x2 trd(LAS char* p) { return __builtin_bit_cast(u32x2, __builtin_amdgcn_ds_read_tr16_b64_v4i16((LAS s16x4*)p)); }
DEV u32x4 lr16(LAS char* p) { return *(LAS u32x4*)p; }
DEV void lw16(LAS char* p, u32x4 v) { *(LAS u32x4*)p = v; }
DEV void lw8(LAS char* p, u32x2 v) { *(LAS u32x2*)p = v; }
DEV u32x4 cat(u32x2 a, u32x2 b) { return u32x4{a.x, a.y, b.x, b.y}; }
DEV u32x8 cat(u32x4 a, u32x4 b) { return u32x8{a.x, a.y, a.z, a.w, b.x, b.y, b.z, b.w}; }
DEV f32x4 mma(u32x4 a, u32x4 b, f32x4 c) {
  return __builtin_amdgcn_mfma_f32_16x16x32_bf16(__builtin_bit_cast(bf8, a), __builtin_bit_cast(bf8, b), c, 0, 0, 0);
}
DEV f32x4 mma16(u32x2 a, u32x2 b, f32x4 c) {
  return __builtin_amdgcn_mfma_f32_16x16x16bf16_1k(__builtin_bit_cast(s16x4, a), __builtin_bit_cast(s16x4, b), c, 0, 0, 0);
}
DEV f32x4 smm(u32x4 a, u32x8 b, f32x4 c, u32 idx) {
  return __builtin_amdgcn_smfmac_f32_16x16x64_bf16(__builtin_bit_cast(bf8, a), __builtin_bit_cast(bf16v, b), c, idx, 0, 0);
}
DEV void bar() { asm volatile("s_waitcnt lgkmcnt(0)\n\ts_barrier" ::: "memory"); }
template <int N> DEV void waitvm() { asm volatile("s_waitcnt vmcnt(%0)" ::"i"(N) : "memory"); }
DEV void waitlds() { asm volatile("s_waitcnt lgkmcnt(0)" ::: "memory"); }
DEV void dma16(const void* g, u32 ldsaddr) {
  u32 keep;
  asm volatile("s_mov_b32 %0, m0\n\ts_mov_b32 m0, %2\n\ts_nop 0\n\tglobal_load_lds_dwordx4 %1, off\n\ts_mov_b32 m0, %0"
               : "=&s"(keep) : "v"(g), "s"(ldsaddr) : "memory");
}
DEV float sw16(float a, bool mx) {
  auto r = __builtin_amdgcn_permlane16_swap(__float_as_uint(a), __float_as_uint(a), false, false);
  float x = __uint_as_float(r[0]), y = __uint_as_float(r[1]);
  return mx ? fmaxf(x, y) : x + y;
}
DEV float sw32(float a, bool mx) {
  auto r = __builtin_amdgcn_permlane32_swap(__float_as_uint(a), __float_as_uint(a), false, false);
  float x = __uint_as_float(r[0]), y = __uint_as_float(r[1]);
  return mx ? fmaxf(x, y) : x + y;
}
template <int C> DEV float ror_add(float u) {
  return u + __int_as_float(__builtin_amdgcn_update_dpp(0, __float_as_int(u), 0x120 + C, 0xf, 0xf, true));
}
DEV f32x4 fold(f32x4 x, f32x4 y) {
  f32x4 o;
#pragma unroll
  for (int i = 0; i < 4; ++i) {
    auto r = __builtin_amdgcn_permlane32_swap(__float_as_uint(x[i]), __float_as_uint(y[i]), false, false);
    o[i] = __uint_as_float(r[0]) + __uint_as_float(r[1]);
  }
  return o;
}
DEV f32x4 vzero() { f32x4 z; asm volatile("v_mov_b32 %0, 0" : "=v"(z[0])); asm volatile("v_mov_b32 %0, 0" : "=v"(z[1])); asm volatile("v_mov_b32 %0, 0" : "=v"(z[2])); asm volatile("v_mov_b32 %0, 0" : "=v"(z[3])); return z; }
DEV u32x2 pk4(f32x4 v) { return u32x2{pk(v[0], v[1]), pk(v[2], v[3])}; }
DEV u32 nosent(u32 v) { return v < 0xfffffffeu ? v : 0xfffffffeu; }
DEV bool valid(u32x4 a) { return a.x != ~0u && a.y != ~0u && a.z != ~0u && a.w != ~0u; }
DEV u32 nearoff(u32 off, u32 side) { return ((off >> 13) << 14) | (off & 8191) | (side << 13); }
DEV u32 memoff(u32 k, u32 quad) {
  u32 hb = (k >> 4) & 1;
  return (k >> 6) * 1024 + hb * 512 + quad * 256 + ((k & 15) ^ (8 * quad)) * 16 + (((k >> 5) & 1) ^ hb) * 8;
}
DEV void delay(int n) { for (int i = 0; i < n; ++i) asm volatile("s_nop 3" ::: "memory"); }
DEV u32 wait_flags(rsrc_t rw, u32 flag, u32 target, bool active) {
  for (u32 spins = 0;; ++spins) {
    asm volatile("" ::: "memory");
    u32 v = ld4(rw, O_CTRL + (active ? flag : F_TMO) * 128, 0);
    if (__all(!active || v >= target)) return __builtin_amdgcn_readfirstlane(v);
    if (spins > SPIN_CAP || __any(!active && v != 0)) { st4(1, rw, O_CTRL + F_TMO * 128, 0); return 0; }
    __builtin_amdgcn_s_sleep(2);
  }
}
DEV void add_flag(char* ws, u32 flag) {
  __hip_atomic_fetch_add((__attribute__((address_space(1))) u32*)(ws + O_CTRL + flag * 128), 1u, __ATOMIC_RELAXED, __HIP_MEMORY_SCOPE_AGENT);
}

__global__ __launch_bounds__(256) void prologue_kernel(Params p) {
  __shared__ float red[4][32];
  __shared__ u32 lat[4];
  const int tid = threadIdx.x, b = blockIdx.x, l = tid & 63, w = tid >> 6;
  rsrc_t rw = mkrsrc(align_ws(p.wsraw), WS_END);
  if (b == 248) {
    u32* q = (u32*)(align_ws(p.wsraw) + O_RING + (w >> 1) * 8192 + (w & 1) * 4096);
    u32 best = ~0u, a = ~0u;
    for (int r = 0; r < 3; ++r) {
      unsigned long long t1, t2; u32 d;
      asm volatile("global_store_dword %0, %1, off sc1\n\ts_waitcnt vmcnt(0)" ::"v"(q), "v"(a) : "memory");
      asm volatile("s_memtime %0\n\ts_waitcnt lgkmcnt(0)\n\tglobal_load_dword %2, %3, off sc1\n\ts_waitcnt vmcnt(0)\n\ts_memtime %1\n\ts_waitcnt lgkmcnt(0)"
                   : "=&s"(t1), "=&s"(t2), "=&v"(d) : "v"(q) : "memory");
      best = min(best, (u32)(t2 - t1));
    }
    lat[w] = best;
    __syncthreads();
    if (tid == 0) st4(lat[0] + lat[1] <= lat[2] + lat[3] ? 0u : 1u, rw, O_CTRL + F_SIDE * 128, 0);
  }
  for (u32 j = b * 256 + tid; j < 2 * MB / 16; j += 65536) {
    u32x4 v = {~0u, ~0u, ~0u, ~0u};
    if (j < 1024) {
      u32 ch = j & 511, hb = (ch >> 5) & 1, quad = (ch >> 4) & 1, kk = (ch >> 6) * 64 + hb * 16 + ((ch & 15) ^ (8 * quad));
      const unsigned short* m = (const unsigned short*)p.mem0 + quad * 4 * 512;
      u32 ka = kk + hb * 32, kb = kk + (hb ^ 1) * 32;
      v = u32x4{nosent(m[ka] | (u32)m[512 + ka] << 16), nosent(m[1024 + ka] | (u32)m[1536 + ka] << 16),
                nosent(m[kb] | (u32)m[512 + kb] << 16), nosent(m[1024 + kb] | (u32)m[1536 + kb] << 16)};
    }
    st16(v, rw, j * 16, 0);
  }
  if (b >= 96 && b < 99) { u32 f = (b - 96) * 256 + tid; if (f < F_END && f != F_SIDE) st4(0, rw, O_CTRL + f * 128, 0); }
  if (b < 96) {
    const int kq = tid >> 2, cq = tid & 3;
    u32x4 cv = *(const u32x4*)(p.c + kq * 8);
    float acc[8] = {0, 0, 0, 0, 0, 0, 0, 0};
#pragma unroll
    for (int i = 0; i < 8; ++i) {
      float cc = (i & 1) ? hi(cv[i >> 1]) : lo(cv[i >> 1]);
      float s = rb(cc / (1.f + __expf(-cc)));
      u32x4 wv = *(const u32x4*)(p.wmod + (size_t)(kq * 8 + i) * 3072 + b * 32 + cq * 8);
#pragma unroll
      for (int j = 0; j < 4; ++j) { acc[2 * j] += s * lo(wv[j]); acc[2 * j + 1] += s * hi(wv[j]); }
    }
#pragma unroll
    for (int j = 0; j < 8; ++j) { float t = sw32(sw16(ror_add<8>(ror_add<4>(acc[j])), false), false); if (l < 4) red[w][l * 8 + j] = t; }
    __syncthreads();
    if (tid < 32) ((float*)(align_ws(p.wsraw) + O_MOD))[b * 32 + tid] = rb(red[0][tid] + red[1][tid] + red[2][tid] + red[3][tid]);
  }
}

constexpr u32 L_MEM = 131072, L_K = L_MEM + 8192, L_Q = L_K + 8192, L_KM = L_Q + 2048, L_O = L_KM + 2048, L_END = L_O + 4096;
#define D1 12
#define D2 12

DEV void stage_qkv(const __bf16* W, int h, int w, int l, int half, u32 dst) {
  const char* g = (const char*)(W + (size_t)(half * 256 + (l >> 2)) * 4096 + h * 128 + w * 32) + (l & 3) * 16;
#pragma unroll
  for (int i = 0; i < 16; ++i) dma16(g + (size_t)i * 16 * 8192, dst + i * 1024);
}
DEV void read_qkv(LAS char* src, int l, u32x8 (&W)[8][2], int half) {
  LAS char* a = src + ((l >> 4) * 8 + ((l >> 2) & 3)) * 64 + (l & 3) * 8;
#pragma unroll
  for (int B = 0; B < 4; ++B)
#pragma unroll
    for (int t = 0; t < 2; ++t) {
      LAS char* q = a + B * 4096 + t * 32;
      W[half * 4 + B][t] = cat(cat(trd(q), trd(q + 256)), cat(trd(q + 2048), trd(q + 2304)));
    }
}

DEV void role_recur(const Params& p, LAS char* sm, int r) {
  const int tid = threadIdx.x, l = tid & 63, w = __builtin_amdgcn_readfirstlane(tid >> 6);
  const int h = (r & 3) + 4 * (r >> 3), c = (r >> 2) & 1, rr = h * 2 + c;
  const int g = l >> 4, n = l & 15, q4 = (l >> 2) & 3, p4 = l & 3;
  rsrc_t rw = mkrsrc(align_ws(p.wsraw), WS_END);
  const u32 side = __builtin_amdgcn_readfirstlane(ld4(rw, O_CTRL + F_SIDE * 128, 0)) & 1;
  const u32 smb = (u32)(size_t)sm;
  LAS char* stA = sm + w * 32768; LAS char* stB = stA + 16384;
  const u32 dA = smb + w * 32768, dB = dA + 16384;
  u32x8 Wq[8][2], Wk[8][2], Wo[4][2];
  stage_qkv(p.wq, h, w, l, 0, dA); stage_qkv(p.wq, h, w, l, 1, dB);
  waitvm<16>(); read_qkv(stA, l, Wq, 0); waitlds(); stage_qkv(p.wk, h, w, l, 0, dA);
  waitvm<16>(); read_qkv(stB, l, Wq, 1); waitlds(); stage_qkv(p.wk, h, w, l, 1, dB);
  waitvm<16>(); read_qkv(stA, l, Wk, 0); waitlds();
  {
    const char* gp = (const char*)(p.wo + (size_t)(h * 128 + (l >> 3)) * 512 + c * 256 + w * 64) + (l & 7) * 16;
#pragma unroll
    for (int i = 0; i < 16; ++i) dma16(gp + i * 8 * 1024, dA + i * 1024);
  }
  waitvm<16>(); read_qkv(stB, l, Wk, 1); waitlds(); stage_qkv(p.wv, h, w, l, 0, dB);
  waitvm<16>();
  {
    LAS char* a = stA + (16 * (g & 1) + 4 * (g >> 1) + (q4 & 1)) * 128 + (q4 >> 1) * 32 + p4 * 8;
#pragma unroll
    for (int eb = 0; eb < 4; ++eb)
#pragma unroll
      for (int t = 0; t < 2; ++t) {
        LAS char* q = a + eb * 4096 + t * 64;
        Wo[eb][t] = cat(cat(trd(q), trd(q + 256)), cat(trd(q + 1024), trd(q + 1280)));
      }
  }
  waitlds(); stage_qkv(p.wv, h, w, l, 1, dA);
#pragma unroll
  for (int half = 0; half < 2; ++half) {
    if (half == 0) waitvm<16>(); else waitvm<0>();
    LAS char* s = half ? stA : stB;
    u32x8 T[8][2];
    read_qkv(s, l, T, 0); waitlds();
#pragma unroll
    for (int B = 0; B < 4; ++B)
#pragma unroll
      for (int t = 0; t < 2; ++t) {
        lw16(s + (B * 4 + t * 2) * 1024 + l * 16, u32x4{T[B][t][0], T[B][t][1], T[B][t][2], T[B][t][3]});
        lw16(s + (B * 4 + t * 2 + 1) * 1024 + l * 16, u32x4{T[B][t][4], T[B][t][5], T[B][t][6], T[B][t][7]});
      }
  }
  const u32 idx = (l & 8) ? 0xEEEEu : 0x4444u;
  u32x4 sel;
#pragma unroll
  for (int j = 0; j < 4; ++j) sel[j] = ((l & 7) == 2 * j ? 0x3f80u : 0u) | ((l & 7) == 2 * j + 1 ? 0x3f800000u : 0u);
  LAS char* aA0 = sm + L_MEM + memoff(16 * g + 4 * (q4 >> 1) + (q4 & 1) + 2 * (p4 >> 1), p4 & 1);
  LAS char* aA1 = sm + L_MEM + memoff(16 * g + 8 + 4 * (q4 >> 1) + (q4 & 1) + 2 * (p4 >> 1), p4 & 1);
  LAS char* aXw = sm + L_Q + w * 512 + (g & 1) * 256 + ((16 * (g >> 1) + n) ^ (4 * (g & 1))) * 8;
  LAS char* aX0 = sm + L_Q + (p4 & 1) * 256 + ((8 * g + q4) ^ (4 * (p4 & 1))) * 8;
  LAS char* aX1 = sm + L_Q + (p4 & 1) * 256 + ((8 * g + 4 + q4) ^ (4 * (p4 & 1))) * 8;
  const u32 vMem = side * 8192 + w * 2048 + l * 16;
  const u32 vPst = nearoff((c * 32 + w * 8 + (l >> 3)) * 4096 + h * 128 + (l & 7) * 16, side);
  const u32 vPld = nearoff(rr * 4096 + (l >> 1) * 128 + w * 32 + (l & 1) * 16, side);
  u32 vMst;
  {
    u32 ch = rr * 8 + w * 2 + (l & 1), gg = (ch >> 4) & 3;
    vMst = side * 8192 + memoff((ch >> 6) * 64 + 32 * (g & 1) + 16 * (gg >> 1) + (ch & 15), gg & 1);
  }
  const u32 vKV = h * 8192 + w * 2048 + l * 16;
  u32 prog = wait_flags(rw, F_KV + h, 1, l == 0);
  if (!prog) return;
  u32x4 kn0 = ld16(rw, vKV, O_K), kn1 = ld16(rw, vKV + 1024, O_K), vn0 = ld16(rw, vKV, O_V), vn1 = ld16(rw, vKV + 1024, O_V);
  const f32x4 z4 = {0.f, 0.f, 0.f, 0.f};
  const float sc = 0.08838834764831845f * 1.4426950408889634f;

  for (int s = 0; s < 64; ++s) {
    lw16(sm + L_K + w * 2048 + l * 16, kn0); lw16(sm + L_K + w * 2048 + 1024 + l * 16, kn1);
    const u32x4 vc0 = vn0, vc1 = vn1;
    f32x4 qx = vzero(), qy = vzero(), kx = vzero(), ky = vzero(), vx = vzero(), vy = vzero(), d0 = vzero(), d1 = vzero();
    if (s) delay(D2);
    u32x4 ma, mb;
    for (u32 spins = 0;; ++spins) {
      asm volatile("" ::: "memory");
      ma = ld16(rw, vMem, O_MEM + s * 16384); mb = ld16(rw, vMem + 1024, O_MEM + s * 16384);
      if (__all(valid(ma) && valid(mb))) break;
      if (spins > SPIN_CAP) { st4(2, rw, O_CTRL + F_TMO * 128, 0); return; }
    }
    lw16(sm + L_MEM + w * 2048 + l * 16, ma); lw16(sm + L_MEM + w * 2048 + 1024 + l * 16, mb);
    bar();
    if (tid == 0 && s) st4(s, rw, O_CTRL + (F_KM + rr) * 128, 0);
    u32x4 af[8];
#pragma unroll
    for (int B = 0; B < 8; ++B) af[B] = cat(trd(aA0 + B * 1024), trd(aA1 + B * 1024));
#pragma unroll
    for (int B = 0; B < 8; ++B) { qx = smm(af[B], Wq[B][0], qx, idx); qy = smm(af[B], Wq[B][1], qy, idx); }
#pragma unroll
    for (int B = 0; B < 8; ++B) { kx = smm(af[B], Wk[B][0], kx, idx); ky = smm(af[B], Wk[B][1], ky, idx); }
    lw8(aXw, pk4(fold(qx, qy)));
    lw8(aXw + 2048, pk4(fold(kx, ky)));
    bar();
    if (s < 63) {
      const u32 s1 = s + 1;
      if (prog < (s1 & 15) + 1 || (s1 & 15) == 0) {
        prog = wait_flags(rw, F_KV + (s1 >> 4) * 48 + h, (s1 & 15) + 1, l == 0);
        if (!prog) return;
      }
      kn0 = ld16(rw, vKV, O_K + s1 * 262144); kn1 = ld16(rw, vKV + 1024, O_K + s1 * 262144);
      vn0 = ld16(rw, vKV, O_V + s1 * 262144); vn1 = ld16(rw, vKV + 1024, O_V + s1 * 262144);
    }
    f32x4 s0 = z4, s1v = z4, sm_ = z4;
#pragma unroll
    for (int eb = 0; eb < 4; ++eb) {
      u32x4 qf = cat(trd(aX0 + eb * 512), trd(aX1 + eb * 512));
      u32x4 kf = cat(trd(aX0 + 2048 + eb * 512), trd(aX1 + 2048 + eb * 512));
      s0 = mma(lr16(sm + L_K + eb * 2048 + l * 16), qf, s0);
      s1v = mma(lr16(sm + L_K + eb * 2048 + 1024 + l * 16), qf, s1v);
      sm_ = mma(kf, qf, sm_);
    }
#pragma unroll
    for (int B = 0; B < 8; ++B) {
      LAS char* q = sm + w * 32768 + ((B & 4) ? 0 : 16384) + (B & 3) * 4096 + l * 16;
      vx = smm(af[B], cat(lr16(q), lr16(q + 1024)), vx, idx);
      vy = smm(af[B], cat(lr16(q + 2048), lr16(q + 3072)), vy, idx);
    }
    const u32x2 vm = pk4(fold(vx, vy));
    if (s < 63) {
      float mx = fmaxf(fmaxf(fmaxf(s0[0], s0[1]), fmaxf(s0[2], s0[3])), fmaxf(fmaxf(s1v[0], s1v[1]), fmaxf(s1v[2], s1v[3])));
      mx = fmaxf(mx, fmaxf(fmaxf(sm_[0], sm_[1]), fmaxf(sm_[2], sm_[3])));
      mx = sw32(sw16(mx, true), true) * sc;
      float sum = 0.f, summ = 0.f;
#pragma unroll
      for (int i = 0; i < 4; ++i) {
        s0[i] = __builtin_amdgcn_exp2f(s0[i] * sc - mx); s1v[i] = __builtin_amdgcn_exp2f(s1v[i] * sc - mx);
        sm_[i] = __builtin_amdgcn_exp2f(sm_[i] * sc - mx);
        sum += s0[i] + s1v[i]; summ += sm_[i];
      }
      const u32x4 pf = cat(pk4(s0), pk4(s1v));
      const u32x2 pm = pk4(sm_), zz = {0u, 0u};
      f32x4 ox = mma(vc0, pf, z4), oy = mma(vc1, pf, z4);
      ox = mma16(vm, g < 2 ? pm : zz, ox); oy = mma16(vm, g < 2 ? zz : pm, oy);
      const float inv = __builtin_amdgcn_rcpf(sw32(sw16(sum + 0.5f * summ, false), false));
      lw16(sm + L_O + w * 1024 + l * 16, cat(pk4(ox * inv), pk4(oy * inv)));
      bar();
#pragma unroll
      for (int eb = 0; eb < 4; ++eb) {
        u32x4 of = lr16(sm + L_O + eb * 1024 + l * 16);
        d0 = smm(of, Wo[eb][0], d0, idx); d1 = smm(of, Wo[eb][1], d1, idx);
      }
      u32x4 o = cat(pk4(d0), pk4(d1));
      st16(u32x4{nosent(o.x), nosent(o.y), nosent(o.z), nosent(o.w)}, rw, vPst, O_RING + (s & 1) * 524288);
    }
    if (c == 0) {
      u32x4 kf = cat(trd(aX0 + 2048 + w * 512), trd(aX1 + 2048 + w * 512));
      if (!(l & 8)) st16(kf, rw, h * 2048 + w * 512 + g * 128 + (l & 7) * 16, O_KM + s * 65536);
    } else st8(vm, rw, h * 2048 + w * 512 + l * 8, O_VM + s * 65536);
    if (s == 63) break;
    delay(D1);
    u32x4 pp;
    for (u32 spins = 0;; ++spins) {
      asm volatile("" ::: "memory");
      pp = ld16(rw, vPld, O_RING + (s & 1) * 524288);
      if (__all(valid(pp))) break;
      if (spins > SPIN_CAP) { st4(3, rw, O_CTRL + F_TMO * 128, 0); return; }
    }
    f32x4 t = mma(sel, pp, z4);
#pragma unroll
    for (int e = 0; e < 4; ++e) t[e] = ror_add<8>(ror_add<4>(ror_add<2>(t[e])));
    u32x2 mo = pk4(t);
    if ((l & 0x2e) == 0) st8(u32x2{nosent(mo.x), nosent(mo.y)}, rw, vMst, O_MEM + (s + 1) * 16384);
    st16(u32x4{~0u, ~0u, ~0u, ~0u}, rw, vPld, O_RING + (s & 1) * 524288);
  }
  waitvm<0>();
  bar();
  if (tid == 0) st4(64, rw, O_CTRL + (F_KM + rr) * 128, 0);
}

template <int ROWS, int RB> DEV void stage_w(const char* g, size_t rowstride, int l, u32 dst) {
  constexpr int LPR = RB / 16, RPI = 64 / LPR;
  const char* a = g + (size_t)(l / LPR) * rowstride + (l % LPR) * 16;
#pragma unroll
  for (int i = 0; i < ROWS / RPI; ++i) dma16(a + (size_t)i * RPI * rowstride, dst + i * 1024);
}
DEV bool wg_wait(rsrc_t rw, LAS char* sm, int w, u32 flag, u32 target, bool active) {
  LAS u32* okw = (LAS u32*)(sm + 155584);
  if (w == 0) *okw = wait_flags(rw, flag, target, active);
  bar();
  const u32 ok = *okw;
  bar();
  return ok != 0;
}

DEV void phase_ln1(const Params& p, rsrc_t rw, LAS char* sm, int wk) {
  const int tid = threadIdx.x, l = tid & 63, w = tid >> 6;
  const float* mod = (const float*)(align_ws(p.wsraw) + O_MOD);
  f32x4 sa0 = *(const f32x4*)(mod + l * 8), sa1 = *(const f32x4*)(mod + l * 8 + 4);
  f32x4 ca0 = *(const f32x4*)(mod + 512 + l * 8), ca1 = *(const f32x4*)(mod + 512 + l * 8 + 4);
  u32x4 xv[4];
#pragma unroll
  for (int i = 0; i < 4; ++i) xv[i] = *(const u32x4*)(p.x + (size_t)(wk * 16 + w * 4 + i) * 512 + l * 8);
#pragma unroll
  for (int i = 0; i < 4; ++i) {
    float v[8], s = 0.f, q = 0.f;
#pragma unroll
    for (int j = 0; j < 4; ++j) { v[2 * j] = lo(xv[i][j]); v[2 * j + 1] = hi(xv[i][j]); s += v[2 * j] + v[2 * j + 1]; }
    s = sw32(sw16(ror_add<8>(ror_add<4>(ror_add<2>(ror_add<1>(s)))), false), false) * (1.f / 512);
#pragma unroll
    for (int j = 0; j < 8; ++j) { v[j] -= s; q += v[j] * v[j]; }
    q = sw32(sw16(ror_add<8>(ror_add<4>(ror_add<2>(ror_add<1>(q)))), false), false) * (1.f / 512);
    const float rs = rsqrtf(q + 1e-5f);
    u32x4 o;
#pragma unroll
    for (int j = 0; j < 4; ++j) {
      const float c0 = j < 2 ? ca0[2 * j] : ca1[2 * j - 4], c1 = j < 2 ? ca0[2 * j + 1] : ca1[2 * j - 3];
      const float a0 = j < 2 ? sa0[2 * j] : sa1[2 * j - 4], a1 = j < 2 ? sa0[2 * j + 1] : sa1[2 * j - 3];
      o[j] = pk(v[2 * j] * rs * (1.f + c0) + a0, v[2 * j + 1] * rs * (1.f + c1) + a1);
    }
    lw16(sm + 131072 + l * 256 + (((w * 4 + i) ^ l) & 15) * 16, o);
  }
  bar();
#pragma unroll
  for (int i = 0; i < 4; ++i) {
    const int ch = i * 256 + tid;
    st16(lr16(sm + 131072 + (ch >> 4) * 256 + ((ch ^ (ch >> 4)) & 15) * 16), rw, wk * 16384 + ch * 16, O_H);
  }
  waitvm<0>();
  bar();
  if (tid == 0) add_flag(align_ws(p.wsraw), F_H + (wk >> 1));
}

template <bool ISV> DEV bool qkv_loop(rsrc_t rw, LAS char* sm, const u32x4 (&W)[16][4], int rg, u32 obase, u32 flag) {
  const int tid = threadIdx.x, l = tid & 63, w = tid >> 6;
  const f32x4 z4 = {0.f, 0.f, 0.f, 0.f};
  u32x4 pf[8];
#pragma unroll
  for (int j = 0; j < 8; ++j) pf[j] = ld16(rw, w * 8192 + j * 1024 + l * 16, O_H + rg * 16 * 32768);
#pragma unroll
  for (int j = 0; j < 8; ++j) lw16(sm + w * 8192 + j * 1024 + l * 16, pf[j]);
  bar();
  for (int i = 0; i < 16; ++i) {
    const int seg = rg * 16 + i;
    if (i < 15) {
#pragma unroll
      for (int j = 0; j < 8; ++j) pf[j] = ld16(rw, w * 8192 + j * 1024 + l * 16, O_H + (seg + 1) * 32768);
    }
    LAS char* a = sm + (i & 1) * 32768 + l * 16;
    f32x4 acc[2][4];
#pragma unroll
    for (int t = 0; t < 4; ++t) { acc[0][t] = z4; acc[1][t] = z4; }
#pragma unroll
    for (int ks = 0; ks < 16; ++ks) {
      const u32x4 h0 = lr16(a + ks * 1024), h1 = lr16(a + 16384 + ks * 1024);
#pragma unroll
      for (int t = 0; t < 4; ++t) {
        acc[0][t] = ISV ? mma(h0, W[ks][t], acc[0][t]) : mma(W[ks][t], h0, acc[0][t]);
        acc[1][t] = ISV ? mma(h1, W[ks][t], acc[1][t]) : mma(W[ks][t], h1, acc[1][t]);
      }
    }
#pragma unroll
    for (int t = 0; t < 4; ++t) {
      const u32x4 o = ISV ? cat(pk4(acc[0][t]), pk4(acc[1][t])) : cat(pk4(acc[t & 1][t & 2]), pk4(acc[t & 1][(t & 2) + 1]));
      st16(o, rw, t * 1024 + l * 16, obase + seg * 262144);
    }
    if (i == 0 || i == 15) waitvm<0>(); else waitvm<4>();
    if (i < 15) {
#pragma unroll
      for (int j = 0; j < 8; ++j) lw16(sm + ((i + 1) & 1) * 32768 + w * 8192 + j * 1024 + l * 16, pf[j]);
    }
    bar();
    if (tid == 0) st4(i == 0 ? 1 : (i == 15 ? 16 : i), rw, O_CTRL + flag * 128, 0);
  }
  return true;
}

DEV bool phase_qkv(const Params& p, rsrc_t rw, LAS char* sm, int wk) {
  const int tid = threadIdx.x, l = tid & 63, w = __builtin_amdgcn_readfirstlane(tid >> 6);
  const int cb = wk % 48, rg = wk / 48, g = l >> 4, q4 = (l >> 2) & 3, p4 = l & 3;
  const bool isv = cb < 32 && w >= 2;
  const int head = cb < 32 ? cb : 2 * (cb - 32) + (w >> 1);
  const __bf16* M = cb >= 32 ? p.wq : (isv ? p.wv : p.wk);
  const char* gsrc = (const char*)(M + head * 128 + (w & 1) * 64);
  const u32 dst = (u32)(size_t)sm + w * 32768;
  stage_w<128, 128>(gsrc, 8192, l, dst); stage_w<128, 128>(gsrc + 128 * 8192, 8192, l, dst + 16384);
  if (wk < 128) phase_ln1(p, rw, sm, wk);
  u32x4 W[16][4];
  LAS char* a = sm + w * 32768 + (8 * g + q4) * 128 + (isv ? p4 * 8 : p4 * 16);
  const int tstep = isv ? 32 : 8;
#pragma unroll
  for (int c = 0; c < 4; ++c) {
    if (c < 3) waitvm<16>(); else waitvm<0>();
#pragma unroll
    for (int k = 0; k < 4; ++k)
#pragma unroll
      for (int t = 0; t < 4; ++t) {
        LAS char* q = a + (c & 1) * 16384 + k * 4096 + (t >> 1) * 64 + (t & 1) * tstep;
        W[c * 4 + k][t] = cat(trd(q), trd(q + 512));
      }
    waitlds();
    if (c < 2) stage_w<128, 128>(gsrc + (size_t)(c + 2) * 128 * 8192, 8192, l, dst + (c & 1) * 16384);
  }
  if (!wg_wait(rw, sm, w, F_H + rg * 16 + l, 2, l < 16)) return false;
  const u32 obase = (cb >= 32 ? O_Q : (isv ? O_V : O_K)) + head * 8192 + (w & 1) * 4096;
  if (isv) qkv_loop<true>(rw, sm, W, rg, obase, F_KV + rg * 48 + cb);
  else qkv_loop<false>(rw, sm, W, rg, obase, F_KV + rg * 48 + cb);
  return true;
}

DEV void role_attn(const Params& p, rsrc_t rw, LAS char* sm, int set, int hp) {
  const int tid = threadIdx.x, l = tid & 63, w = __builtin_amdgcn_readfirstlane(tid >> 6);
  const int g = l >> 4, n = l & 15, q4 = (l >> 2) & 3, p4 = l & 3;
  const f32x4 z4 = {0.f, 0.f, 0.f, 0.f};
  const char* gsrc = (const char*)(p.wo + (size_t)hp * 256 * 512 + w * 128);
  const u32 dst = (u32)(size_t)sm + w * 32768;
  u32x4 W[8][8];
  stage_w<64, 256>(gsrc, 1024, l, dst); stage_w<64, 256>(gsrc + 64 * 1024, 1024, l, dst + 16384);
  LAS char* a = sm + w * 32768 + (4 * g + q4) * 256 + p4 * 16;
#pragma unroll
  for (int c = 0; c < 4; ++c) {
    if (c < 3) waitvm<16>(); else waitvm<0>();
#pragma unroll
    for (int k = 0; k < 2; ++k)
#pragma unroll
      for (int t = 0; t < 8; ++t) {
        LAS char* q = a + (c & 1) * 16384 + k * 8192 + (t >> 1) * 64 + (t & 1) * 8;
        W[c * 2 + k][t] = cat(trd(q), trd(q + 4096));
      }
    waitlds();
    if (c < 2) stage_w<64, 256>(gsrc + (c + 2) * 64 * 1024, 1024, l, dst + (c & 1) * 16384);
  }
  const int hd = hp * 2 + (w >> 1), qt = w & 1;
  const float sc = 0.08838834764831845f * 1.4426950408889634f;
  for (int s = set; s < 64; s += 3) {
    {
      const u32 rgb = F_KV + (s >> 4) * 48;
      const u32 f = l < 4 ? F_KM + hp * 4 + l : (l < 6 ? rgb + hp * 2 + (l - 4) : rgb + 32 + hp);
      if (!wg_wait(rw, sm, w, f, l < 4 ? s + 1 : (s & 15) + 1, l < 7)) return;
    }
    const u32 vo = hd * 8192 + l * 16, so = s * 262144;
    u32x4 kf[4][2], qf[4], km[4], vf[4][2]; u32x2 vm[4];
#pragma unroll
    for (int eb = 0; eb < 4; ++eb) {
      qf[eb] = ld16(rw, vo + eb * 2048 + qt * 1024, O_Q + so);
      kf[eb][0] = ld16(rw, vo + eb * 2048, O_K + so); kf[eb][1] = ld16(rw, vo + eb * 2048 + 1024, O_K + so);
      km[eb] = ld16(rw, hd * 2048 + eb * 512 + g * 128 + (l & 7) * 16, O_KM + s * 65536);
    }
#pragma unroll
    for (int eb = 0; eb < 4; ++eb) {
      vf[eb][0] = ld16(rw, vo + eb * 2048, O_V + so); vf[eb][1] = ld16(rw, vo + eb * 2048 + 1024, O_V + so);
      vm[eb] = ld8(rw, hd * 2048 + eb * 512 + l * 8, O_VM + s * 65536);
    }
    f32x4 s0 = z4, s1 = z4, sm_ = z4;
#pragma unroll
    for (int eb = 0; eb < 4; ++eb) { s0 = mma(kf[eb][0], qf[eb], s0); s1 = mma(kf[eb][1], qf[eb], s1); sm_ = mma(km[eb], qf[eb], sm_); }
    float mx = -3.0e38f;
#pragma unroll
    for (int i = 0; i < 4; ++i) {
      s0[i] = (4 * g + i <= 16 * qt + n) ? s0[i] * sc : -1.4426950408889634e9f;
      s1[i] = (16 + 4 * g + i <= 16 * qt + n) ? s1[i] * sc : -1.4426950408889634e9f;
      sm_[i] *= sc;
      mx = fmaxf(mx, fmaxf(fmaxf(s0[i], s1[i]), sm_[i]));
    }
    mx = sw32(sw16(mx, true), true);
    float sum = 0.f, summ = 0.f;
#pragma unroll
    for (int i = 0; i < 4; ++i) {
      s0[i] = __builtin_amdgcn_exp2f(s0[i] - mx); s1[i] = __builtin_amdgcn_exp2f(s1[i] - mx); sm_[i] = __builtin_amdgcn_exp2f(sm_[i] - mx);
      sum += s0[i] + s1[i]; summ += sm_[i];
    }
    const float inv = __builtin_amdgcn_rcpf(sw32(sw16(sum + 0.5f * summ, false), false));
    const u32x4 pf = cat(pk4(s0), pk4(s1));
    const u32x2 pm = pk4(sm_), zz = {0u, 0u}, pmx = g < 2 ? pm : zz, pmy = g < 2 ? zz : pm;
#pragma unroll
    for (int eb = 0; eb < 4; ++eb) {
      const f32x4 ox = mma16(vm[eb], pmx, mma(vf[eb][0], pf, z4)), oy = mma16(vm[eb], pmy, mma(vf[eb][1], pf, z4));
      lw16(sm + (qt * 8 + (w >> 1) * 4 + eb) * 1024 + l * 16, cat(pk4(ox * inv), pk4(oy * inv)));
    }
    bar();
#pragma unroll
    for (int t2 = 0; t2 < 2; ++t2) {
      f32x4 acc[8];
#pragma unroll
      for (int t = 0; t < 8; ++t) acc[t] = z4;
#pragma unroll
      for (int ks = 0; ks < 8; ++ks) {
        const u32x4 of = lr16(sm + (t2 * 8 + ks) * 1024 + l * 16);
#pragma unroll
        for (int t = 0; t < 8; ++t) acc[t] = mma(W[ks][t], of, acc[t]);
      }
#pragma unroll
      for (int u = 0; u < 4; ++u)
        st16(cat(pk4(acc[2 * u]), pk4(acc[2 * u + 1])), rw, hp * 32768 + t2 * 16384 + (w * 4 + u) * 1024 + l * 16, O_P1 + s * 524288);
    }
    waitvm<0>();
    bar();
    if (tid == 0) add_flag(align_ws(p.wsraw), F_P1 + s);
  }
}

DEV void role_norm(const Params& p, rsrc_t rw, LAS char* sm, int set, int j) {
  const int tid = threadIdx.x, l = tid & 63, w = __builtin_amdgcn_readfirstlane(tid >> 6), t4 = tid & 3, pc = tid >> 2;
  const float* mod = (const float*)(align_ws(p.wsraw) + O_MOD) + pc * 8;
  float ga[8], sb[8], cb[8];
#pragma unroll
  for (int i = 0; i < 8; ++i) { ga[i] = mod[1024 + i]; sb[i] = mod[1536 + i]; cb[i] = 1.f + mod[2048 + i]; }
  LAS float* red = (LAS float*)sm;
  const u32 vo = (j >> 2) * 16384 + pc * 256 + ((4 * j + t4) & 15) * 16;
  for (int s = set; s < 64; s += 3) {
    const u32x4 xv = *(const u32x4*)(p.x + (size_t)(s * 32 + 4 * j + t4) * 512 + pc * 8);
    if (!wg_wait(rw, sm, w, F_P1 + s, 16, l < 1)) return;
    u32x4 pp[16];
#pragma unroll
    for (int i = 0; i < 16; ++i) pp[i] = ld16(rw, vo + i * 32768, O_P1 + s * 524288);
    float v[8] = {0, 0, 0, 0, 0, 0, 0, 0};
#pragma unroll
    for (int i = 0; i < 16; ++i)
#pragma unroll
      for (int e = 0; e < 4; ++e) { v[2 * e] += lo(pp[i][e]); v[2 * e + 1] += hi(pp[i][e]); }
    u32x4 x1; float su = 0.f, q = 0.f;
#pragma unroll
    for (int e = 0; e < 4; ++e) {
      x1[e] = pk(lo(xv[e]) + ga[2 * e] * rb(v[2 * e]), hi(xv[e]) + ga[2 * e + 1] * rb(v[2 * e + 1]));
      v[2 * e] = lo(x1[e]); v[2 * e + 1] = hi(x1[e]); su += v[2 * e] + v[2 * e + 1];
    }
    st16(x1, rw, vo, O_X1 + s * 32768);
    su = sw32(sw16(ror_add<8>(ror_add<4>(su)), false), false);
    if (l < 4) red[w * 4 + l] = su;
    bar();
    su = (red[t4] + red[4 + t4] + red[8 + t4] + red[12 + t4]) * (1.f / 512);
#pragma unroll
    for (int e = 0; e < 8; ++e) { v[e] -= su; q += v[e] * v[e]; }
    q = sw32(sw16(ror_add<8>(ror_add<4>(q)), false), false);
    if (l < 4) red[16 + w * 4 + l] = q;
    bar();
    const float rs = rsqrtf((red[16 + t4] + red[20 + t4] + red[24 + t4] + red[28 + t4]) * (1.f / 512) + 1e-5f);
    u32x4 o;
#pragma unroll
    for (int e = 0; e < 4; ++e) o[e] = pk(v[2 * e] * rs * cb[2 * e] + sb[2 * e], v[2 * e + 1] * rs * cb[2 * e + 1] + sb[2 * e + 1]);
    st16(o, rw, vo, O_H2 + s * 32768);
    waitvm<0>();
    bar();
    if (tid == 0) add_flag(align_ws(p.wsraw), F_H2 + s);
  }
}

DEV float gelu(float x) {
  const float u = x + 0.044715f * x * x * x;
  return x * __builtin_amdgcn_rcpf(1.f + __builtin_amdgcn_exp2f(u * (-2.f * 0.7978845608028654f * 1.4426950408889634f)));
}
DEV void role_mlp(const Params& p, rsrc_t rw, LAS char* sm, int set, int fs) {
  const int tid = threadIdx.x, l = tid & 63, w = __builtin_amdgcn_readfirstlane(tid >> 6);
  const int g = l >> 4, q4 = (l >> 2) & 3, p4 = l & 3;
  const f32x4 z4 = {0.f, 0.f, 0.f, 0.f};
  const bool pad = fs == 10 && w == 3;
  const int f0 = pad ? 0 : fs * 128 + w * 32;
  const u32 dst = (u32)(size_t)sm + w * 32768;
  u32x4 W1[16][2], W2[4][8];
  const char* g1 = (const char*)(p.w1 + f0);
  const char* g2 = (const char*)(p.w2 + (size_t)fs * 128 * 512 + w * 128);
  stage_w<256, 64>(g1, 2752, l, dst); stage_w<256, 64>(g1 + 256 * 2752, 2752, l, dst + 16384);
#pragma unroll
  for (int c = 0; c < 2; ++c) {
    waitvm<16>();
    LAS char* a = sm + w * 32768 + c * 16384 + (8 * g + q4) * 64 + p4 * 16;
#pragma unroll
    for (int k = 0; k < 8; ++k)
#pragma unroll
      for (int t = 0; t < 2; ++t) W1[c * 8 + k][t] = cat(trd(a + k * 2048 + t * 8), trd(a + k * 2048 + t * 8 + 256));
    waitlds();
    stage_w<32, 256>(g2 + c * 64 * 1024, 1024, l, dst + c * 16384);
    stage_w<32, 256>(g2 + ((c == 1 && fs == 10) ? 0 : (c * 64 + 32) * 1024), 1024, l, dst + c * 16384 + 8192);
  }
#pragma unroll
  for (int c = 0; c < 2; ++c) {
    if (c == 0) waitvm<16>(); else waitvm<0>();
    LAS char* a = sm + w * 32768 + c * 16384 + (8 * g + q4) * 256 + p4 * 16;
#pragma unroll
    for (int k = 0; k < 2; ++k)
#pragma unroll
      for (int t = 0; t < 8; ++t) {
        LAS char* q = a + k * 8192 + (t >> 1) * 64 + (t & 1) * 8;
        W2[c * 2 + k][t] = cat(trd(q), trd(q + 1024));
      }
  }
  if (fs == 10) {
#pragma unroll
    for (int t = 0; t < 8; ++t) W2[3][t] = u32x4{0u, 0u, 0u, 0u};
  }
  const u32x4 b1v = *(const u32x4*)(p.b1 + f0 + g * 8);
  for (int s = set; s < 64; s += 3) {
    if (!wg_wait(rw, sm, w, F_H2 + s, 8, l < 1)) return;
    u32x4 pf[8];
#pragma unroll
    for (int j = 0; j < 8; ++j) pf[j] = ld16(rw, w * 8192 + j * 1024 + l * 16, O_H2 + s * 32768);
#pragma unroll
    for (int j = 0; j < 8; ++j) lw16(sm + w * 8192 + j * 1024 + l * 16, pf[j]);
    bar();
#pragma unroll
    for (int t2 = 0; t2 < 2; ++t2) {
      f32x4 a0 = {lo(b1v[0]), hi(b1v[0]), lo(b1v[1]), hi(b1v[1])}, a1 = {lo(b1v[2]), hi(b1v[2]), lo(b1v[3]), hi(b1v[3])};
#pragma unroll
      for (int ks = 0; ks < 16; ++ks) {
        const u32x4 hf = lr16(sm + t2 * 16384 + ks * 1024 + l * 16);
        a0 = mma(W1[ks][0], hf, a0); a1 = mma(W1[ks][1], hf, a1);
      }
#pragma unroll
      for (int i = 0; i < 4; ++i) { a0[i] = pad ? 0.f : gelu(rb(a0[i])); a1[i] = pad ? 0.f : gelu(rb(a1[i])); }
      lw16(sm + 32768 + (t2 * 4 + w) * 1024 + l * 16, cat(pk4(a0), pk4(a1)));
    }
    bar();
#pragma unroll
    for (int t2 = 0; t2 < 2; ++t2) {
      f32x4 acc[8];
#pragma unroll
      for (int t = 0; t < 8; ++t) acc[t] = z4;
#pragma unroll
      for (int ks = 0; ks < 4; ++ks) {
        const u32x4 gf = lr16(sm + 32768 + (t2 * 4 + ks) * 1024 + l * 16);
#pragma unroll
        for (int t = 0; t < 8; ++t) acc[t] = mma(W2[ks][t], gf, acc[t]);
      }
#pragma unroll
      for (int u = 0; u < 4; ++u)
        st16(cat(pk4(acc[2 * u]), pk4(acc[2 * u + 1])), rw, fs * 32768 + t2 * 16384 + (w * 4 + u) * 1024 + l * 16, O_P2 + s * 360448);
    }
    waitvm<0>();
    bar();
    if (tid == 0) add_flag(align_ws(p.wsraw), F_P2 + s);
  }
}

DEV void role_out(const Params& p, rsrc_t rw, LAS char* sm, int set, int j) {
  const int tid = threadIdx.x, l = tid & 63, w = __builtin_amdgcn_readfirstlane(tid >> 6), t4 = tid & 3, pc = tid >> 2;
  const float* mod = (const float*)(align_ws(p.wsraw) + O_MOD) + pc * 8;
  float gb[8];
#pragma unroll
  for (int i = 0; i < 8; ++i) gb[i] = mod[2560 + i];
  const u32x4 b2v = *(const u32x4*)(p.b2 + pc * 8);
  const u32 vo = (j >> 2) * 16384 + pc * 256 + ((4 * j + t4) & 15) * 16;
  for (int s = set; s < 64; s += 3) {
    if (!wg_wait(rw, sm, w, F_P2 + s, 11, l < 1)) return;
    u32x4 pp[11];
#pragma unroll
    for (int i = 0; i < 11; ++i) pp[i] = ld16(rw, vo + i * 32768, O_P2 + s * 360448);
    const u32x4 x1 = ld16(rw, vo, O_X1 + s * 32768);
    u32x4 o;
#pragma unroll
    for (int e = 0; e < 4; ++e) {
      float a = 0.f, b = 0.f;
#pragma unroll
      for (int i = 0; i < 11; ++i) { a += lo(pp[i][e]); b += hi(pp[i][e]); }
      o[e] = pk(lo(x1[e]) + gb[2 * e] * rb(rb(a) + lo(b2v[e])), hi(x1[e]) + gb[2 * e + 1] * rb(rb(b) + hi(b2v[e])));
    }
    *(u32x4*)(p.out + (size_t)(s * 32 + 4 * j + t4) * 512 + pc * 8) = o;
  }
}

__global__ __launch_bounds__(256, 1) void main_kernel(Params p) {
  __shared__ __attribute__((aligned(16))) char smem[155648 + 64];
  LAS char* sm = (LAS char*)smem;
  const int b = blockIdx.x, i4 = (b >> 3) * 4 + (b & 3);
  if ((b & 4) == 0 && b < 128) { role_recur(p, sm, i4); return; }
  const int wk = (b & 4) ? i4 : 64 + i4;
  rsrc_t rw = mkrsrc(align_ws(p.wsraw), WS_END);
  if (!phase_qkv(p, rw, sm, wk)) return;
  if (wk < 48) role_attn(p, rw, sm, wk / 16, wk % 16);
  else if (wk < 81) role_mlp(p, rw, sm, (wk - 48) / 11, (wk - 48) % 11);
  else if (wk < 105) role_norm(p, rw, sm, (wk - 81) / 8, (wk - 81) % 8);
  else if (wk >= 128 && wk < 152) role_out(p, rw, sm, (wk - 128) / 8, (wk - 128) % 8);
}

extern "C" void kernel_launch(void* const* d_in, const int* in_sizes, int n_in, void* d_out, int out_size, void* d_ws,
                              size_t ws_size, hipStream_t stream) {
  Params p;
  p.x = (const __bf16*)d_in[0]; p.mem0 = (const __bf16*)d_in[1]; p.wq = (const __bf16*)d_in[2]; p.wk = (const __bf16*)d_in[3];
  p.wv = (const __bf16*)d_in[4]; p.wo = (const __bf16*)d_in[5]; p.w1 = (const __bf16*)d_in[8]; p.b1 = (const __bf16*)d_in[9];
  p.w2 = (const __bf16*)d_in[10]; p.b2 = (const __bf16*)d_in[11]; p.c = (const __bf16*)d_in[12]; p.wmod = (const __bf16*)d_in[13];
  p.out = (__bf16*)d_out;
  p.wsraw = (char*)d_ws;
  prologue_kernel<<<256, 256, 0, stream>>>(p);
  main_kernel<<<256, 256, 0, stream>>>(p);
}
```

```cpp
#include <hip/hip_runtime.h>
#include <hip/hip_bf16.h>

typedef unsigned u32;
typedef unsigned u32x2 __attribute__((ext_vector_type(2)));
typedef unsigned u32x4 __attribute__((ext_vector_type(4)));
typedef unsigned u32x8 __attribute__((ext_vector_type(8)));
typedef float f32x2 __attribute__((ext_vector_type(2)));
typedef float f32x4 __attribute__((ext_vector_type(4)));
typedef short s16x4 __attribute__((ext_vector_type(4)));
typedef __bf16 bf2 __attribute__((ext_vector_type(2)));
typedef __bf16 bf8 __attribute__((ext_vector_type(8)));
typedef __bf16 bf16v __attribute__((ext_vector_type(16)));
typedef __amdgpu_buffer_rsrc_t rsrc_t;
#define LAS __attribute__((address_space(3)))
#define DEV __device__ __forceinline__
constexpr int SC1 = 16;
constexpr u32 SPIN_CAP = 400000;

constexpr u32 MB = 1u << 20;
constexpr u32 O_MEM = 0;
constexpr u32 O_RING = 1 * MB;
constexpr u32 O_CTRL = 2 * MB;
constexpr u32 O_MOD = 2 * MB + 131072;
constexpr u32 O_H = 4 * MB, O_X1 = 6 * MB, O_H2 = 8 * MB, O_KM = 10 * MB, O_VM = 14 * MB;
constexpr u32 O_Q = 18 * MB, O_K = 34 * MB, O_V = 50 * MB, O_P1 = 66 * MB, O_P2 = 98 * MB, WS_END = 120 * MB;
constexpr u32 F_TMO = 0, F_SIDE = 1, F_H = 2, F_KV = F_H + 64, F_KM = F_KV + 192, F_P1 = F_KM + 64, F_H2 = F_P1 + 64,
              F_P2 = F_H2 + 64, F_END = F_P2 + 64;

struct Params {
  const __bf16 *x, *mem0, *wq, *wk, *wv, *wo, *w1, *b1, *w2, *b2, *c, *wmod;
  __bf16* out;
  char* wsraw;
};

DEV char* align_ws(char* p) { return p + ((0 - (size_t)p) & (2 * MB - 1)); }
DEV rsrc_t mkrsrc(const void* p, u32 n) { return __builtin_amdgcn_make_buffer_rsrc((void*)p, 0, n, 0x00020000); }
DEV u32x4 ld16(rsrc_t r, u32 v, u32 s) { return __builtin_amdgcn_raw_buffer_load_b128(r, v, s, SC1); }
DEV u32x2 ld8(rsrc_t r, u32 v, u32 s) { return __builtin_amdgcn_raw_buffer_load_b64(r, v, s, SC1); }
DEV u32 ld4(rsrc_t r, u32 v, u32 s) { return __builtin_amdgcn_raw_buffer_load_b32(r, v, s, SC1); }
DEV void st16(u32x4 d, rsrc_t r, u32 v, u32 s) { __builtin_amdgcn_raw_buffer_store_b128(d, r, v, s, SC1); }
DEV void st8(u32x2 d, rsrc_t r, u32 v, u32 s) { __builtin_amdgcn_raw_buffer_store_b64(d, r, v, s, SC1); }
DEV void st4(u32 d, rsrc_t r, u32 v, u32 s) { __builtin_amdgcn_raw_buffer_store_b32(d, r, v, s, SC1); }
DEV u32 pk(float a, float b) { f32x2 v = {a, b}; return __builtin_bit_cast(u32, __builtin_convertvector(v, bf2)); }
DEV float lo(u32 v) { return __uint_as_float(v << 16); }
DEV float hi(u32 v) { return __uint_as_float(v & 0xffff0000u); }
DEV float rb(float a) { return lo(pk(a, a)); }
DEV u32x2 trd(LAS char* p) { return __builtin_bit_cast(u32x2, __builtin_amdgcn_ds_read_tr16_b64_v4i16((LAS s16x4*)p)); }
DEV u32x4 lr16(LAS char* p) { return *(LAS u32x4*)p; }
DEV void lw16(LAS char* p, u32x4 v) { *(LAS u32x4*)p = v; }
DEV void lw8(LAS char* p, u32x2 v) { *(LAS u32x2*)p = v; }
DEV u32x4 cat(u32x2 a, u32x2 b) { return u32x4{a.x, a.y, b.x, b.y}; }
DEV u32x8 cat(u32x4 a, u32x4 b) { return u32x8{a.x, a.y, a.z, a.w, b.x, b.y, b.z, b.w}; }
DEV f32x4 mma(u32x4 a, u32x4 b, f32x4 c) {
  return __builtin_amdgcn_mfma_f32_16x16x32_bf16(__builtin_bit_cast(bf8, a), __builtin_bit_cast(bf8, b), c, 0, 0, 0);
}
DEV f32x4 mma16(u32x2 a, u32x2 b, f32x4 c) {
  return __builtin_amdgcn_mfma_f32_16x16x16bf16_1k(__builtin_bit_cast(s16x4, a), __builtin_bit_cast(s16x4, b), c, 0, 0, 0);
}
DEV f32x4 smm(u32x4 a, u32x8 b, f32x4 c, u32 idx) {
  return __builtin_amdgcn_smfmac_f32_16x16x64_bf16(__builtin_bit_cast(bf8, a), __builtin_bit_cast(bf16v, b), c, idx, 0, 0);
}
#define MEMB asm volatile("" ::: "memory")
DEV float max3(float a, float b, float c) { float r; asm("v_max3_f32 %0, %1, %2, %3" : "=v"(r) : "v"(a), "v"(b), "v"(c)); return r; }
DEV void bar() { asm volatile("s_waitcnt lgkmcnt(0)\n\ts_barrier" ::: "memory"); }
template <int N> DEV void waitvm() { asm volatile("s_waitcnt vmcnt(%0)" ::"i"(N) : "memory"); }
DEV void waitlds() { asm volatile("s_waitcnt lgkmcnt(0)" ::: "memory"); }
DEV void dma16(const void* g, u32 ldsaddr) {
  u32 keep;
  asm volatile("s_mov_b32 %0, m0\n\ts_mov_b32 m0, %2\n\ts_nop 0\n\tglobal_load_lds_dwordx4 %1, off\n\ts_mov_b32 m0, %0"
               : "=&s"(keep) : "v"(g), "s"(ldsaddr) : "memory");
}
DEV float sw16(float a, bool mx) {
  auto r = __builtin_amdgcn_permlane16_swap(__float_as_uint(a), __float_as_uint(a), false, false);
  float x = __uint_as_float(r[0]), y = __uint_as_float(r[1]);
  return mx ? fmaxf(x, y) : x + y;
}
DEV float sw32(float a, bool mx) {
  auto r = __builtin_amdgcn_permlane32_swap(__float_as_uint(a), __float_as_uint(a), false, false);
  float x = __uint_as_float(r[0]), y = __uint_as_float(r[1]);
  return mx ? fmaxf(x, y) : x + y;
}
template <int C> DEV float ror_add(float u) {
  return u + __int_as_float(__builtin_amdgcn_update_dpp(0, __float_as_int(u), 0x120 + C, 0xf, 0xf, true));
}
DEV f32x4 fold(f32x4 x, f32x4 y) {
  f32x4 o;
#pragma unroll
  for (int i = 0; i < 4; ++i) {
    auto r = __builtin_amdgcn_permlane32_swap(__float_as_uint(x[i]), __float_as_uint(y[i]), false, false);
    o[i] = __uint_as_float(r[0]) + __uint_as_float(r[1]);
  }
  return o;
}
DEV f32x4 vzero() { f32x4 z; asm volatile("v_mov_b32 %0, 0" : "=v"(z[0])); asm volatile("v_mov_b32 %0, 0" : "=v"(z[1])); asm volatile("v_mov_b32 %0, 0" : "=v"(z[2])); asm volatile("v_mov_b32 %0, 0" : "=v"(z[3])); return z; }
DEV u32x2 pk4(f32x4 v) { return u32x2{pk(v[0], v[1]), pk(v[2], v[3])}; }
DEV u32 nosent(u32 v) { return v < 0xfffffffeu ? v : 0xfffffffeu; }
DEV bool valid(u32x4 a) { return a.x != ~0u && a.y != ~0u && a.z != ~0u && a.w != ~0u; }
DEV u32 nearoff(u32 off, u32 side) { return ((off >> 13) << 14) | (off & 8191) | (side << 13); }
DEV u32 memoff(u32 k, u32 quad) {
  u32 hb = (k >> 4) & 1;
  return (k >> 6) * 1024 + hb * 512 + quad * 256 + ((k & 15) ^ (8 * quad)) * 16 + (((k >> 5) & 1) ^ hb) * 8;
}
template <int N> DEV void delay() {
#pragma unroll
  for (int i = 0; i < N; ++i) asm volatile("s_nop 7" ::: "memory");
}
DEV u32 umax3(u32 a, u32 b, u32 c) { u32 r; asm("v_max3_u32 %0, %1, %2, %3" : "=v"(r) : "v"(a), "v"(b), "v"(c)); return r; }
DEV float fadd(float a, float b) { float r; asm("v_add_f32 %0, %1, %2" : "=v"(r) : "v"(a), "v"(b)); return r; }
DEV float fmul(float a, float b) { float r; asm("v_mul_f32 %0, %1, %2" : "=v"(r) : "v"(a), "v"(b)); return r; }
DEV f32x4 add4(f32x4 a, f32x4 b) { return f32x4{fadd(a[0], b[0]), fadd(a[1], b[1]), fadd(a[2], b[2]), fadd(a[3], b[3])}; }
DEV f32x4 mul4(f32x4 a, float b) { return f32x4{fmul(a[0], b), fmul(a[1], b), fmul(a[2], b), fmul(a[3], b)}; }
DEV void halt() { asm volatile("s_endpgm" ::: "memory"); __builtin_unreachable(); }
DEV bool nosentinel(u32 m) { return __builtin_amdgcn_ballot_w64(m == ~0u) == 0; }
DEV u32 wait_flags(rsrc_t rw, u32 flag, u32 target, bool active) {
  for (u32 spins = 0;; ++spins) {
    asm volatile("" ::: "memory");
    u32 v = ld4(rw, O_CTRL + (active ? flag : F_TMO) * 128, 0);
    if (__all(!active || v >= target)) return __builtin_amdgcn_readfirstlane(v);
    if (spins > SPIN_CAP || __any(!active && v != 0)) { st4(1, rw, O_CTRL + F_TMO * 128, 0); return 0; }
    __builtin_amdgcn_s_sleep(2);
  }
}
#define STAMP(i)
#define PROFV(i, v)
#define CYC(i)
DEV void add_flag(char* ws, u32 flag) {
  __hip_atomic_fetch_add((__attribute__((address_space(1))) u32*)(ws + O_CTRL + flag * 128), 1u, __ATOMIC_RELAXED, __HIP_MEMORY_SCOPE_AGENT);
}

#define NPROBE 3
__global__ __launch_bounds__(256) void prologue_kernel(Params p) {
  __shared__ float red[4][32];
  const int tid = threadIdx.x, b = blockIdx.x, l = tid & 63, w = tid >> 6;
  if (b == 0) STAMP(0);
  rsrc_t rw = mkrsrc(align_ws(p.wsraw), WS_END);
  for (u32 j = b * 256 + tid; j < 2 * MB / 16; j += 65536) {
    u32x4 v = {~0u, ~0u, ~0u, ~0u};
    if (j < 1024) {
      u32 ch = j & 511, hb = (ch >> 5) & 1, quad = (ch >> 4) & 1, kk = (ch >> 6) * 64 + hb * 16 + ((ch & 15) ^ (8 * quad));
      const unsigned short* m = (const unsigned short*)p.mem0 + quad * 4 * 512;
      u32 ka = kk + hb * 32, kb = kk + (hb ^ 1) * 32;
      v = u32x4{nosent(m[ka] | (u32)m[512 + ka] << 16), nosent(m[1024 + ka] | (u32)m[1536 + ka] << 16),
                nosent(m[kb] | (u32)m[512 + kb] << 16), nosent(m[1024 + kb] | (u32)m[1536 + kb] << 16)};
    }
    st16(v, rw, j * 16, 0);
  }
  if (b >= 96 && b < 99) { u32 f = (b - 96) * 256 + tid; if (f < F_END) st4(0, rw, O_CTRL + f * 128, 0); }
  if (b < 96) {
    const int kq = tid >> 2, cq = tid & 3;
    u32x4 cv = *(const u32x4*)(p.c + kq * 8);
    float acc[8] = {0, 0, 0, 0, 0, 0, 0, 0};
#pragma unroll
    for (int i = 0; i < 8; ++i) {
      float cc = (i & 1) ? hi(cv[i >> 1]) : lo(cv[i >> 1]);
      float s = rb(cc / (1.f + __expf(-cc)));
      u32x4 wv = *(const u32x4*)(p.wmod + (size_t)(kq * 8 + i) * 3072 + b * 32 + cq * 8);
#pragma unroll
      for (int j = 0; j < 4; ++j) { acc[2 * j] += s * lo(wv[j]); acc[2 * j + 1] += s * hi(wv[j]); }
    }
#pragma unroll
    for (int j = 0; j < 8; ++j) { float t = sw32(sw16(ror_add<8>(ror_add<4>(acc[j])), false), false); if (l < 4) red[w][l * 8 + j] = t; }
    __syncthreads();
    if (tid < 32) ((float*)(align_ws(p.wsraw) + O_MOD))[b * 32 + tid] = rb(red[0][tid] + red[1][tid] + red[2][tid] + red[3][tid]);
  }
}

constexpr u32 L_MEM = 131072, L_K = L_MEM + 8192, L_Q = L_K + 8192, L_KM = L_Q + 2048, L_O = L_KM + 2048, L_V = L_O + 4096, L_END = L_V + 8192;
#define D1 8
#define D2 17

DEV void stage_qkv(const __bf16* W, int h, int w, int l, int half, u32 dst) {
  const char* g = (const char*)(W + (size_t)(half * 256 + (l >> 2)) * 4096 + h * 128 + w * 32) + (l & 3) * 16;
#pragma unroll
  for (int i = 0; i < 16; ++i) dma16(g + (size_t)i * 16 * 8192, dst + i * 1024);
}
DEV void read_qkv(LAS char* src, int l, u32x8 (&W)[8][2], int half) {
  LAS char* a = src + ((l >> 4) * 8 + ((l >> 2) & 3)) * 64 + (l & 3) * 8;
#pragma unroll
  for (int B = 0; B < 4; ++B)
#pragma unroll
    for (int t = 0; t < 2; ++t) {
      LAS char* q = a + B * 4096 + t * 32;
      W[half * 4 + B][t] = cat(cat(trd(q), trd(q + 256)), cat(trd(q + 2048), trd(q + 2304)));
    }
}

DEV void role_recur(const Params& p, LAS char* sm, int r) {
  const int tid = threadIdx.x, l = tid & 63, w = __builtin_amdgcn_readfirstlane(tid >> 6);
  const int h = (r & 3) + 4 * (r >> 3), c = (r >> 2) & 1, rr = h * 2 + c;
  const int g = l >> 4, n = l & 15, q4 = (l >> 2) & 3, p4 = l & 3;
  rsrc_t rw = mkrsrc(align_ws(p.wsraw), WS_END);
  if (r == 0) {
    const char* q = align_ws(p.wsraw) + O_MEM + 4096 + w * 1024;
    u32 best[2] = {~0u, ~0u}, d;
    asm volatile("global_load_dword %0, %1, off sc1\n\ts_waitcnt vmcnt(0)" : "=v"(d) : "v"(q - 4096) : "memory");
#pragma unroll 1
    for (int i = 0; i < NPROBE; ++i)
#pragma unroll
      for (int e = 0; e < 2; ++e) {
        unsigned long long t1, t2;
        asm volatile("s_memtime %0\n\ts_waitcnt lgkmcnt(0)\n\tglobal_load_dword %2, %3, off sc1\n\ts_waitcnt vmcnt(0)\n\ts_memtime %1\n\ts_waitcnt lgkmcnt(0)"
                     : "=&s"(t1), "=&s"(t2), "=&v"(d) : "v"(q + e * 8192 + i * 256) : "memory");
        best[e] = min(best[e], (u32)(t2 - t1));
      }
    LAS u32* lat = (LAS u32*)(sm + L_MEM);
    lat[w * 2] = best[0]; lat[w * 2 + 1] = best[1];
    bar();
    const u32 l0 = min(min(lat[0], lat[2]), min(lat[4], lat[6])), l1 = min(min(lat[1], lat[3]), min(lat[5], lat[7]));
    if (tid == 0) st4(l0 <= l1 ? 1u : 2u, rw, O_CTRL + F_SIDE * 128, 0);
    PROFV(11, l0 * 10000 + l1);
    STAMP(12);
  }
  const u32 smb = (u32)(size_t)sm;
  LAS char* stA = sm + w * 32768; LAS char* stB = stA + 16384;
  const u32 dA = smb + w * 32768, dB = dA + 16384;
  if (r == 0) STAMP(1);
  u32 np1 = 0, np2 = 0;
  u32x8 Wq[8][2], Wv[8][2], Wk[8][2];
  stage_qkv(p.wq, h, w, l, 0, dA); stage_qkv(p.wq, h, w, l, 1, dB);
  waitvm<16>(); read_qkv(stA, l, Wq, 0); waitlds(); stage_qkv(p.wv, h, w, l, 0, dA);
  waitvm<16>(); read_qkv(stB, l, Wq, 1); waitlds(); stage_qkv(p.wv, h, w, l, 1, dB);
  waitvm<16>(); read_qkv(stA, l, Wv, 0); waitlds(); stage_qkv(p.wk, h, w, l, 0, dA);
  waitvm<16>(); read_qkv(stB, l, Wv, 1); waitlds(); stage_qkv(p.wk, h, w, l, 1, dB);
  waitvm<16>(); read_qkv(stA, l, Wk, 0); waitlds();
  {
    const char* gp = (const char*)(p.wo + (size_t)(h * 128 + (l >> 3)) * 512 + c * 256 + w * 64) + (l & 7) * 16;
#pragma unroll
    for (int i = 0; i < 16; ++i) dma16(gp + i * 8 * 1024, dA + i * 1024);
  }
#pragma unroll
  for (int half = 0; half < 2; ++half) {
    u32x8 T[8][2];
    LAS char* dst = half ? stA : stB;
    if (half == 0) { waitvm<16>(); read_qkv(stB, l, T, 0); }
    else {
      waitvm<0>();
      LAS char* a = stA + (16 * (g & 1) + 4 * (g >> 1) + (q4 & 1)) * 128 + (q4 >> 1) * 32 + p4 * 8;
#pragma unroll
      for (int eb = 0; eb < 4; ++eb)
#pragma unroll
        for (int t = 0; t < 2; ++t) {
          LAS char* q = a + eb * 4096 + t * 64;
          T[eb][t] = cat(cat(trd(q), trd(q + 256)), cat(trd(q + 1024), trd(q + 1280)));
        }
    }
    waitlds();
#pragma unroll
    for (int B = 0; B < 4; ++B)
#pragma unroll
      for (int t = 0; t < 2; ++t) {
        lw16(dst + (B * 4 + t * 2) * 1024 + l * 16, u32x4{T[B][t][0], T[B][t][1], T[B][t][2], T[B][t][3]});
        lw16(dst + (B * 4 + t * 2 + 1) * 1024 + l * 16, u32x4{T[B][t][4], T[B][t][5], T[B][t][6], T[B][t][7]});
      }
  }
  u32 side = wait_flags(rw, F_SIDE, 1, l == 0);
  if (!side) halt();
  side = (side - 1) & 1;
  const u32 idx = (l & 8) ? 0xEEEEu : 0x4444u;
  u32x4 sel;
#pragma unroll
  for (int j = 0; j < 4; ++j) sel[j] = ((l & 7) == 2 * j ? 0x3f80u : 0u) | ((l & 7) == 2 * j + 1 ? 0x3f800000u : 0u);
  LAS char* aA0 = sm + L_MEM + memoff(16 * g + 4 * (q4 >> 1) + (q4 & 1) + 2 * (p4 >> 1), p4 & 1);
  LAS char* aA1 = sm + L_MEM + memoff(16 * g + 8 + 4 * (q4 >> 1) + (q4 & 1) + 2 * (p4 >> 1), p4 & 1);
  LAS char* aXw = sm + L_Q + w * 512 + (g & 1) * 256 + ((16 * (g >> 1) + n) ^ (4 * (g & 1))) * 8;
  LAS char* aX0 = sm + L_Q + (p4 & 1) * 256 + ((8 * g + q4) ^ (4 * (p4 & 1))) * 8;
  LAS char* aX1 = sm + L_Q + (p4 & 1) * 256 + ((8 * g + 4 + q4) ^ (4 * (p4 & 1))) * 8;
  const u32 vMem = side * 8192 + w * 2048 + l * 16;
  const u32 vPst = nearoff((c * 32 + w * 8 + (l >> 3)) * 4096 + h * 128 + (l & 7) * 16, side);
  const u32 vPld = nearoff(rr * 4096 + (l >> 1) * 128 + w * 32 + (l & 1) * 16, side);
  u32 vMst;
  {
    u32 ch = rr * 8 + w * 2 + (l & 1), gg = (ch >> 4) & 3;
    vMst = side * 8192 + memoff((ch >> 6) * 64 + 32 * (g & 1) + 16 * (gg >> 1) + (ch & 15), gg & 1);
  }
  const u32 vKV = h * 8192 + w * 2048 + l * 16, vKV2 = vKV + 1024;
  if (r == 0) STAMP(2);
  u32 prog = wait_flags(rw, F_KV + h, 1, l == 0);
  if (!prog) halt();
  u32x4 kn0 = ld16(rw, vKV, O_K), kn1 = ld16(rw, vKV + 1024, O_K), vn0 = ld16(rw, vKV, O_V), vn1 = ld16(rw, vKV + 1024, O_V);
  const f32x4 z4 = {0.f, 0.f, 0.f, 0.f};
  const float sc = 0.08838834764831845f * 1.4426950408889634f;

  LAS char* aWo = stA + l * 16; LAS char* aWk = stB + l * 16;
  LAS char* aK = sm + L_K + l * 16; LAS char* aO = sm + L_O + l * 16; LAS char* aM = sm + L_MEM + w * 2048 + l * 16;
  LAS char* aV = sm + L_V + w * 2048 + l * 16;
#define PIN(x) asm volatile("" : "+v"(x))
#define PIN2(x, y) asm volatile("" : "+v"(x), "+v"(y))
#define NOPD(n, x, y) asm volatile("s_nop " #n : "+v"(x), "+v"(y))
#define SMA(d, a, b) asm volatile("v_smfmac_f32_16x16x64_bf16 %0, %1, %2, %3" : "+v"(d) : "v"(a), "a"(b), "v"(idx))
#define SMV(d, a, b) asm volatile("v_smfmac_f32_16x16x64_bf16 %0, %1, %2, %3" : "+v"(d) : "v"(a), "v"(b), "v"(idx))
#define MM0(d, a, b) asm volatile("v_mfma_f32_16x16x32_bf16 %0, %1, %2, 0" : "=&v"(d) : "v"(a), "v"(b))
#define MMA(d, a, b) asm volatile("v_mfma_f32_16x16x32_bf16 %0, %1, %2, %0" : "+v"(d) : "v"(a), "v"(b))
#define M16(d, a, b) asm volatile("v_mfma_f32_16x16x16_bf16 %0, %1, %2, %0" : "+v"(d) : "v"(a), "v"(b))
#define SWP(x, y, i) do { auto r_ = __builtin_amdgcn_permlane32_swap(__float_as_uint(x[i]), __float_as_uint(y[i]), false, false); \
    x[i] = __uint_as_float(r_[0]); y[i] = __uint_as_float(r_[1]); } while (0)
#define LW(a, i) cat(lr16((a) + (i) * 2048), lr16((a) + (i) * 2048 + 1024))
#define PUBLISH(s) do { if (c == 0) { const u32x4 kmw = cat(trd(aX0 + 2048 + w * 512), trd(aX1 + 2048 + w * 512)); \
      if (!(l & 8)) st16(kmw, rw, h * 2048 + w * 512 + g * 128 + (l & 7) * 16, O_KM + (s) * 65536); \
    } else st8(vm, rw, h * 2048 + w * 512 + l * 8, O_VM + (s) * 65536); } while (0)
#define LDA(d, v, so) asm volatile("buffer_load_dwordx4 %0, %1, %2, %3 offen sc1" : "=&v"(d) : "v"(v), "s"(rw), "s"(so) : "memory")
#define EX(x) __builtin_amdgcn_exp2f(__builtin_fmaf(x, sc, mx))
  lw16(aK + w * 2048, kn0); lw16(aK + w * 2048 + 1024, kn1);
  lw16(aV, vn0); lw16(aV + 1024, vn1);
  for (int s = 0;; ++s) {
    f32x4 qx = z4, qy = z4, kx = z4, ky = z4;
    PIN2(qx, qy); PIN2(kx, ky);
    Wk[4][0] = LW(aWk, 0); Wk[4][1] = LW(aWk, 1); Wk[5][0] = LW(aWk, 2); Wk[5][1] = LW(aWk, 3);
    delay<D2>();
    CYC(0);
    bool ok;
    u32 spins = 0;
#pragma unroll 1
    do {
      MEMB;
      const u32x4 ma = ld16(rw, vMem, O_MEM + s * 16384), mb = ld16(rw, vMem + 1024, O_MEM + s * 16384);
      ++np2;
      lw16(aM, ma); lw16(aM + 1024, mb);
      ok = nosentinel(umax3(umax3(ma.x, ma.y, ma.z), umax3(mb.x, mb.y, mb.z), umax3(ma.w, mb.w, mb.w)));
    } while (!ok && ++spins < SPIN_CAP);
    if (!ok) { st4(2, rw, O_CTRL + F_TMO * 128, 0); halt(); }
    CYC(1);
    bar();
    CYC(2);
    u32x4 af[8];
#pragma unroll
    for (int B = 0; B < 8; ++B) { af[B] = cat(trd(aA0 + B * 1024), trd(aA1 + B * 1024)); MEMB; }
    SMA(qx, af[0], Wq[0][0]); SMA(qy, af[0], Wq[0][1]); SMA(qx, af[1], Wq[1][0]); MEMB;
    const u32 s1 = s < 63 ? s + 1 : 63;
    if (prog < s1 + 1) {
      prog = wait_flags(rw, F_KV + (s1 >> 4) * 48 + h, (s1 & 15) + 1, l == 0);
      if (!prog) halt();
      prog += s1 & 48;
    }
    LDA(kn0, vKV, O_K + s1 * 262144);
    SMA(qy, af[1], Wq[1][1]);
#pragma unroll
    for (int B = 2; B < 8; ++B) {
      SMA(qx, af[B], Wq[B][0]); SMA(qy, af[B], Wq[B][1]);
      if (B >= 2 && B < 6) { MEMB; Wk[6 + ((B - 2) >> 1)][B & 1] = LW(aWk, B + 2); MEMB; }
    }
#pragma unroll
    for (int B = 4; B < 7; ++B) {
      SMV(kx, af[B], Wk[B][0]);
      if (B == 4) LDA(kn1, vKV2, O_K + s1 * 262144);
      SMV(ky, af[B], Wk[B][1]);
    }
    NOPD(1, qx, qy);
    SWP(qx, qy, 0); SWP(qx, qy, 1); PIN2(qx, qy);
    SMV(kx, af[7], Wk[7][0]);
    SWP(qx, qy, 2); SWP(qx, qy, 3); PIN2(qx, qy);
    SMV(ky, af[7], Wk[7][1]);
    qx = add4(qx, qy); PIN(qx);
    SMV(kx, af[0], Wk[0][0]);
    { u32x2 t2 = pk4(qx); PIN(t2); SMV(ky, af[0], Wk[0][1]); MEMB; lw8(aXw, t2); MEMB; }
    u32x4 qf[4], kf[4][2];
    kf[0][0] = lr16(aK); kf[0][1] = lr16(aK + 1024); MEMB;
    SMV(kx, af[1], Wk[1][0]); MEMB;
    LDA(vn0, vKV, O_V + s1 * 262144);
    kf[1][0] = lr16(aK + 2048); kf[1][1] = lr16(aK + 3072); MEMB;
    SMV(ky, af[1], Wk[1][1]);
    asm volatile("s_waitcnt lgkmcnt(4)\n\ts_barrier" ::: "memory");
    qf[0] = cat(trd(aX0), trd(aX1)); MEMB;
    SMV(kx, af[2], Wk[2][0]); MEMB;
    qf[1] = cat(trd(aX0 + 512), trd(aX1 + 512)); MEMB;
    SMV(ky, af[2], Wk[2][1]); MEMB;
    qf[2] = cat(trd(aX0 + 1024), trd(aX1 + 1024)); kf[2][0] = lr16(aK + 4096); MEMB;
    SMV(kx, af[3], Wk[3][0]); MEMB;
    qf[3] = cat(trd(aX0 + 1536), trd(aX1 + 1536)); kf[2][1] = lr16(aK + 5120); MEMB;
    SMV(ky, af[3], Wk[3][1]); MEMB;
    kf[3][0] = lr16(aK + 6144); kf[3][1] = lr16(aK + 7168); MEMB;
    f32x4 s0, s1v, vx = z4, vy = z4;
    PIN2(vx, vy);
    MM0(s0, kf[0][0], qf[0]); MM0(s1v, kf[0][1], qf[0]);
    NOPD(3, kx, ky);
    SWP(kx, ky, 0); SWP(kx, ky, 1); PIN2(kx, ky);
    MMA(s0, kf[1][0], qf[1]);
    SWP(kx, ky, 2); SWP(kx, ky, 3); PIN2(kx, ky);
    MMA(s1v, kf[1][1], qf[1]);
    kx = add4(kx, ky); PIN(kx);
    MMA(s0, kf[2][0], qf[2]);
    { u32x2 t2 = pk4(kx); PIN(t2); MMA(s1v, kf[2][1], qf[2]); MEMB; lw8(aXw + 2048, t2); MEMB; }
    MMA(s0, kf[3][0], qf[3]); MMA(s1v, kf[3][1], qf[3]);
    SMA(vx, af[0], Wv[0][0]); MEMB;
    LDA(vn1, vKV2, O_V + s1 * 262144);
    bar();
    CYC(3);
    u32x4 km[4];
    km[0] = cat(trd(aX0 + 2048), trd(aX1 + 2048)); km[1] = cat(trd(aX0 + 2560), trd(aX1 + 2560)); MEMB;
    SMA(vy, af[0], Wv[0][1]); MEMB;
    km[2] = cat(trd(aX0 + 3072), trd(aX1 + 3072)); km[3] = cat(trd(aX0 + 3584), trd(aX1 + 3584)); MEMB;
    SMA(vx, af[1], Wv[1][0]); MEMB;
    const u32x4 vc0 = lr16(aV), vc1 = lr16(aV + 1024); MEMB;
    SMA(vy, af[1], Wv[1][1]);
    PIN2(s0, s1v);
    float mx = max3(max3(s0[0], s0[1], s0[2]), max3(s1v[0], s1v[1], s1v[2]), max3(s0[3], s1v[3], s1v[3]));
    PIN(mx);
    f32x4 sma, smb_;
    MM0(sma, km[0], qf[0]); MM0(smb_, km[1], qf[1]); MMA(sma, km[2], qf[2]); MMA(smb_, km[3], qf[3]);
    SMA(vx, af[2], Wv[2][0]); SMA(vy, af[2], Wv[2][1]);
    u32x8 Wo[4][2];
    NOPD(0, sma, smb_);
    sma = add4(sma, smb_);
    mx = max3(max3(mx, sma[0], sma[1]), sma[2], sma[3]);
    PIN(mx);
    SMA(vx, af[3], Wv[3][0]);
    { auto r_ = __builtin_amdgcn_permlane16_swap(__float_as_uint(mx), __float_as_uint(mx), false, false); mx = max3(__uint_as_float(r_[0]), __uint_as_float(r_[1]), __uint_as_float(r_[1])); }
    { auto r_ = __builtin_amdgcn_permlane32_swap(__float_as_uint(mx), __float_as_uint(mx), false, false); mx = max3(__uint_as_float(r_[0]), __uint_as_float(r_[1]), __uint_as_float(r_[1])) * -sc; }
    PIN(mx);
    SMA(vy, af[3], Wv[3][1]); MEMB;
#pragma unroll
    for (int i = 0; i < 4; ++i) {
      s0[i] = EX(s0[i]); s1v[i] = EX(s1v[i]); PIN2(s0, s1v);
      SMA(vx, af[4 + i], Wv[4 + i][0]);
      sma[i] = EX(sma[i]); PIN(sma);
      SMA(vy, af[4 + i], Wv[4 + i][1]);
    }
    u32x4 pf = cat(pk4(s0), pk4(s1v));
    u32x2 pm = pk4(sma), pmx, pmy;
    pmx.x = g < 2 ? pm.x : 0u; pmx.y = g < 2 ? pm.y : 0u; pmy.x = g < 2 ? 0u : pm.x; pmy.y = g < 2 ? 0u : pm.y;
    PIN(pf); PIN2(pmx, pmy); PIN2(vx, vy);
    f32x4 ox, oy;
    MM0(ox, vc0, pf);
    SWP(vx, vy, 0); SWP(vx, vy, 1); PIN2(vx, vy);
    MM0(oy, vc1, pf);
    SWP(vx, vy, 2); SWP(vx, vy, 3);
    u32x2 vm = pk4(add4(vx, vy));
    s0 = add4(s0, s1v);
    PIN(vm); PIN(s0);
    M16(ox, vm, pmx); M16(oy, vm, pmy); MEMB;
    Wo[0][0] = LW(aWo, 0); Wo[0][1] = LW(aWo, 1); MEMB;
    float inv = __builtin_amdgcn_rcpf(sw32(sw16(((s0[0] + s0[1]) + (s0[2] + s0[3])) + 0.5f * ((sma[0] + sma[1]) + (sma[2] + sma[3])), false), false));
    PIN(inv); PIN2(ox, oy);
    CYC(4);
    if (s == 63) { asm volatile("s_waitcnt vmcnt(0)" :: "v"(kn0), "v"(kn1), "v"(vn0), "v"(vn1) : "memory"); PUBLISH(63); break; }
    {
      lw16(aO + w * 1024, cat(pk4(mul4(ox, inv)), pk4(mul4(oy, inv)))); MEMB;
      Wo[1][0] = LW(aWo, 2); Wo[1][1] = LW(aWo, 3);
      f32x4 d0 = z4, d1 = z4;
      PIN2(d0, d1);
      asm volatile("s_waitcnt lgkmcnt(4)\n\ts_barrier" ::: "memory");
      CYC(5);
      u32x4 of[4];
#pragma unroll
      for (int eb = 0; eb < 4; ++eb) of[eb] = lr16(aO + eb * 1024);
      MEMB;
      Wo[2][0] = LW(aWo, 4); Wo[2][1] = LW(aWo, 5); Wo[3][0] = LW(aWo, 6); Wo[3][1] = LW(aWo, 7);
      MEMB;
#pragma unroll
      for (int eb = 0; eb < 4; ++eb) { SMV(d0, of[eb], Wo[eb][0]); SMV(d1, of[eb], Wo[eb][1]); }
      asm volatile("s_nop 6" : "+v"(d0));
      u32x2 o0 = pk4(d0); PIN(o0); PIN(d1);
      u32x2 o1 = pk4(d1);
      st16(u32x4{nosent(o0.x), nosent(o0.y), nosent(o1.x), nosent(o1.y)}, rw, vPst, O_RING + (s & 1) * 524288);
      MEMB;
      CYC(6);
      asm volatile("s_waitcnt vmcnt(1)\n\tds_write_b128 %4, %0\n\tds_write_b128 %4, %1 offset:1024\n\tds_write_b128 %5, %2\n\tds_write_b128 %5, %3 offset:1024"
                   :: "v"(kn0), "v"(kn1), "v"(vn0), "v"(vn1), "v"(aK + w * 2048), "v"(aV) : "memory");
    }
    if (r == 0) STAMP(16 + s);
    PUBLISH(s);
    if (tid == 0) st4(s, rw, O_CTRL + (F_KM + rr) * 128, 0);
    if (s < 62 && prog < s + 3) {
      const u32 v = __builtin_amdgcn_readfirstlane(ld4(rw, O_CTRL + (F_KV + ((s + 2) >> 4) * 48 + h) * 128, 0));
      if (v && prog >= ((s + 2) & 48)) prog = v + ((s + 2) & 48);
    } else delay<D1>();
    CYC(7);
    f32x4 t;
    spins = 0;
#pragma unroll 1
    do {
      MEMB;
      const u32x4 pp = ld16(rw, vPld, O_RING + (s & 1) * 524288);
      ++np1;
      MM0(t, sel, pp);
      ok = nosentinel(umax3(umax3(pp.x, pp.y, pp.z), pp.w, pp.w));
    } while (!ok && ++spins < SPIN_CAP);
    if (!ok) { st4(3, rw, O_CTRL + F_TMO * 128, 0); halt(); }
    CYC(8);
#define RA(i, n) "v_add_f32_dpp %" #i ", %" #i ", %" #i " row_ror:" #n " row_mask:0xf bank_mask:0xf\n\t"
    asm volatile("s_nop 3\n\t" RA(0, 2) RA(1, 2) RA(2, 2) RA(3, 2) RA(0, 4) RA(1, 4) RA(2, 4) RA(3, 4) RA(0, 8) RA(1, 8) RA(2, 8) RA(3, 8)
                 : "+v"(t[0]), "+v"(t[1]), "+v"(t[2]), "+v"(t[3]));
    u32x2 mo = pk4(t);
    if ((l & 0x2e) == 0) st8(u32x2{nosent(mo.x), nosent(mo.y)}, rw, vMst, O_MEM + (s + 1) * 16384);
    MEMB;
    CYC(9);
    st16(u32x4{~0u, ~0u, ~0u, ~0u}, rw, vPld, O_RING + (s & 1) * 524288);
  }
  waitvm<0>();
  bar();
  if (r == 0) { STAMP(3); STAMP(79); PROFV(8, np1); PROFV(9, np2); PROFV(10, side); }
  if (tid == 0) st4(64, rw, O_CTRL + (F_KM + rr) * 128, 0);
}

template <int ROWS, int RB> DEV void stage_w(const char* g, size_t rowstride, int l, u32 dst) {
  constexpr int LPR = RB / 16, RPI = 64 / LPR;
  const char* a = g + (size_t)(l / LPR) * rowstride + (l % LPR) * 16;
#pragma unroll
  for (int i = 0; i < ROWS / RPI; ++i) dma16(a + (size_t)i * RPI * rowstride, dst + i * 1024);
}
DEV bool wg_wait(rsrc_t rw, LAS char* sm, int w, u32 flag, u32 target, bool active) {
  LAS u32* okw = (LAS u32*)(sm + 155584);
  if (w == 0) *okw = wait_flags(rw, flag, target, active);
  bar();
  const u32 ok = *okw;
  bar();
  return ok != 0;
}

DEV void phase_ln1(const Params& p, rsrc_t rw, LAS char* sm, int wk) {
  const int tid = threadIdx.x, l = tid & 63, w = tid >> 6;
  const float* mod = (const float*)(align_ws(p.wsraw) + O_MOD);
  f32x4 sa0 = *(const f32x4*)(mod + l * 8), sa1 = *(const f32x4*)(mod + l * 8 + 4);
  f32x4 ca0 = *(const f32x4*)(mod + 512 + l * 8), ca1 = *(const f32x4*)(mod + 512 + l * 8 + 4);
  u32x4 xv[4];
#pragma unroll
  for (int i = 0; i < 4; ++i) xv[i] = *(const u32x4*)(p.x + (size_t)(wk * 16 + w * 4 + i) * 512 + l * 8);
#pragma unroll
  for (int i = 0; i < 4; ++i) {
    float v[8], s = 0.f, q = 0.f;
#pragma unroll
    for (int j = 0; j < 4; ++j) { v[2 * j] = lo(xv[i][j]); v[2 * j + 1] = hi(xv[i][j]); s += v[2 * j] + v[2 * j + 1]; }
    s = sw32(sw16(ror_add<8>(ror_add<4>(ror_add<2>(ror_add<1>(s)))), false), false) * (1.f / 512);
#pragma unroll
    for (int j = 0; j < 8; ++j) { v[j] -= s; q += v[j] * v[j]; }
    q = sw32(sw16(ror_add<8>(ror_add<4>(ror_add<2>(ror_add<1>(q)))), false), false) * (1.f / 512);
    const float rs = rsqrtf(q + 1e-5f);
    u32x4 o;
#pragma unroll
    for (int j = 0; j < 4; ++j) {
      const float c0 = j < 2 ? ca0[2 * j] : ca1[2 * j - 4], c1 = j < 2 ? ca0[2 * j + 1] : ca1[2 * j - 3];
      const float a0 = j < 2 ? sa0[2 * j] : sa1[2 * j - 4], a1 = j < 2 ? sa0[2 * j + 1] : sa1[2 * j - 3];
      o[j] = pk(v[2 * j] * rs * (1.f + c0) + a0, v[2 * j + 1] * rs * (1.f + c1) + a1);
    }
    lw16(sm + 131072 + l * 256 + (((w * 4 + i) ^ l) & 15) * 16, o);
  }
  bar();
#pragma unroll
  for (int i = 0; i < 4; ++i) {
    const int ch = i * 256 + tid;
    st16(lr16(sm + 131072 + (ch >> 4) * 256 + ((ch ^ (ch >> 4)) & 15) * 16), rw, wk * 16384 + ch * 16, O_H);
  }
  waitvm<0>();
  bar();
  if (tid == 0) add_flag(align_ws(p.wsraw), F_H + (wk >> 1));
}

template <bool ISV> DEV bool qkv_loop(rsrc_t rw, LAS char* sm, const u32x4 (&W)[16][4], int rg, u32 obase, u32 flag) {
  const int tid = threadIdx.x, l = tid & 63, w = tid >> 6;
  const f32x4 z4 = {0.f, 0.f, 0.f, 0.f};
  u32x4 pf[8];
#pragma unroll
  for (int j = 0; j < 8; ++j) pf[j] = ld16(rw, w * 8192 + j * 1024 + l * 16, O_H + rg * 16 * 32768);
#pragma unroll
  for (int j = 0; j < 8; ++j) lw16(sm + w * 8192 + j * 1024 + l * 16, pf[j]);
  bar();
  for (int i = 0; i < 16; ++i) {
    const int seg = rg * 16 + i;
    if (i < 15) {
#pragma unroll
      for (int j = 0; j < 8; ++j) pf[j] = ld16(rw, w * 8192 + j * 1024 + l * 16, O_H + (seg + 1) * 32768);
    }
    LAS char* a = sm + (i & 1) * 32768 + l * 16;
    f32x4 acc[2][4];
#pragma unroll
    for (int t = 0; t < 4; ++t) { acc[0][t] = z4; acc[1][t] = z4; }
#pragma unroll
    for (int ks = 0; ks < 16; ++ks) {
      const u32x4 h0 = lr16(a + ks * 1024), h1 = lr16(a + 16384 + ks * 1024);
#pragma unroll
      for (int t = 0; t < 4; ++t) {
        acc[0][t] = ISV ? mma(h0, W[ks][t], acc[0][t]) : mma(W[ks][t], h0, acc[0][t]);
        acc[1][t] = ISV ? mma(h1, W[ks][t], acc[1][t]) : mma(W[ks][t], h1, acc[1][t]);
      }
    }
#pragma unroll
    for (int t = 0; t < 4; ++t) {
      const u32x4 o = ISV ? cat(pk4(acc[0][t]), pk4(acc[1][t])) : cat(pk4(acc[t & 1][t & 2]), pk4(acc[t & 1][(t & 2) + 1]));
      st16(o, rw, t * 1024 + l * 16, obase + seg * 262144);
    }
    if (i == 0 || i == 15) waitvm<0>(); else waitvm<4>();
    if (i < 15) {
#pragma unroll
      for (int j = 0; j < 8; ++j) lw16(sm + ((i + 1) & 1) * 32768 + w * 8192 + j * 1024 + l * 16, pf[j]);
    }
    bar();
    if (tid == 0) st4(i == 0 ? 1 : (i == 15 ? 16 : i), rw, O_CTRL + flag * 128, 0);
  }
  return true;
}

DEV bool phase_qkv(const Params& p, rsrc_t rw, LAS char* sm, int wk) {
  const int tid = threadIdx.x, l = tid & 63, w = __builtin_amdgcn_readfirstlane(tid >> 6);
  const int cb = wk % 48, rg = wk / 48, g = l >> 4, q4 = (l >> 2) & 3, p4 = l & 3;
  const bool isv = cb < 32 && w >= 2;
  const int head = cb < 32 ? cb : 2 * (cb - 32) + (w >> 1);
  const __bf16* M = cb >= 32 ? p.wq : (isv ? p.wv : p.wk);
  const char* gsrc = (const char*)(M + head * 128 + (w & 1) * 64);
  const u32 dst = (u32)(size_t)sm + w * 32768;
  stage_w<128, 128>(gsrc, 8192, l, dst); stage_w<128, 128>(gsrc + 128 * 8192, 8192, l, dst + 16384);
  if (wk < 128) phase_ln1(p, rw, sm, wk);
  u32x4 W[16][4];
  LAS char* a = sm + w * 32768 + (8 * g + q4) * 128 + (isv ? p4 * 8 : p4 * 16);
  const int tstep = isv ? 32 : 8;
#pragma unroll
  for (int c = 0; c < 4; ++c) {
    if (c < 3) waitvm<16>(); else waitvm<0>();
#pragma unroll
    for (int k = 0; k < 4; ++k)
#pragma unroll
      for (int t = 0; t < 4; ++t) {
        LAS char* q = a + (c & 1) * 16384 + k * 4096 + (t >> 1) * 64 + (t & 1) * tstep;
        W[c * 4 + k][t] = cat(trd(q), trd(q + 512));
      }
    waitlds();
    if (c < 2) stage_w<128, 128>(gsrc + (size_t)(c + 2) * 128 * 8192, 8192, l, dst + (c & 1) * 16384);
  }
  if (wk == 0) STAMP(4);
  if (!wg_wait(rw, sm, w, F_H + rg * 16 + l, 2, l < 16)) return false;
  if (wk == 0) STAMP(5);
  const u32 obase = (cb >= 32 ? O_Q : (isv ? O_V : O_K)) + head * 8192 + (w & 1) * 4096;
  if (isv) qkv_loop<true>(rw, sm, W, rg, obase, F_KV + rg * 48 + cb);
  else qkv_loop<false>(rw, sm, W, rg, obase, F_KV + rg * 48 + cb);
  return true;
}

DEV void role_attn(const Params& p, rsrc_t rw, LAS char* sm, int set, int hp) {
  const int tid = threadIdx.x, l = tid & 63, w = __builtin_amdgcn_readfirstlane(tid >> 6);
  const int g = l >> 4, n = l & 15, q4 = (l >> 2) & 3, p4 = l & 3;
  const f32x4 z4 = {0.f, 0.f, 0.f, 0.f};
  const char* gsrc = (const char*)(p.wo + (size_t)hp * 256 * 512 + w * 128);
  const u32 dst = (u32)(size_t)sm + w * 32768;
  u32x4 W[8][8];
  stage_w<64, 256>(gsrc, 1024, l, dst); stage_w<64, 256>(gsrc + 64 * 1024, 1024, l, dst + 16384);
  LAS char* a = sm + w * 32768 + (4 * g + q4) * 256 + p4 * 16;
#pragma unroll
  for (int c = 0; c < 4; ++c) {
    if (c < 3) waitvm<16>(); else waitvm<0>();
#pragma unroll
    for (int k = 0; k < 2; ++k)
#pragma unroll
      for (int t = 0; t < 8; ++t) {
        LAS char* q = a + (c & 1) * 16384 + k * 8192 + (t >> 1) * 64 + (t & 1) * 8;
        W[c * 2 + k][t] = cat(trd(q), trd(q + 4096));
      }
    waitlds();
    if (c < 2) stage_w<64, 256>(gsrc + (c + 2) * 64 * 1024, 1024, l, dst + (c & 1) * 16384);
  }
  const int hd = hp * 2 + (w >> 1), qt = w & 1;
  const float sc = 0.08838834764831845f * 1.4426950408889634f;
  for (int s = set; s < 64; s += 3) {
    {
      const u32 rgb = F_KV + (s >> 4) * 48;
      const u32 f = l < 4 ? F_KM + hp * 4 + l : (l < 6 ? rgb + hp * 2 + (l - 4) : rgb + 32 + hp);
      if (!wg_wait(rw, sm, w, f, l < 4 ? s + 1 : (s & 15) + 1, l < 7)) return;
    }
    const u32 vo = hd * 8192 + l * 16, so = s * 262144;
    u32x4 kf[4][2], qf[4], km[4], vf[4][2]; u32x2 vm[4];
#pragma unroll
    for (int eb = 0; eb < 4; ++eb) {
      qf[eb] = ld16(rw, vo + eb * 2048 + qt * 1024, O_Q + so);
      kf[eb][0] = ld16(rw, vo + eb * 2048, O_K + so); kf[eb][1] = ld16(rw, vo + eb * 2048 + 1024, O_K + so);
      km[eb] = ld16(rw, hd * 2048 + eb * 512 + g * 128 + (l & 7) * 16, O_KM + s * 65536);
    }
#pragma unroll
    for (int eb = 0; eb < 4; ++eb) {
      vf[eb][0] = ld16(rw, vo + eb * 2048, O_V + so); vf[eb][1] = ld16(rw, vo + eb * 2048 + 1024, O_V + so);
      vm[eb] = ld8(rw, hd * 2048 + eb * 512 + l * 8, O_VM + s * 65536);
    }
    f32x4 s0 = z4, s1 = z4, sm_ = z4;
#pragma unroll
    for (int eb = 0; eb < 4; ++eb) { s0 = mma(kf[eb][0], qf[eb], s0); s1 = mma(kf[eb][1], qf[eb], s1); sm_ = mma(km[eb], qf[eb], sm_); }
    float mx = -3.0e38f;
#pragma unroll
    for (int i = 0; i < 4; ++i) {
      s0[i] = (4 * g + i <= 16 * qt + n) ? s0[i] * sc : -1.4426950408889634e9f;
      s1[i] = (16 + 4 * g + i <= 16 * qt + n) ? s1[i] * sc : -1.4426950408889634e9f;
      sm_[i] *= sc;
      mx = fmaxf(mx, fmaxf(fmaxf(s0[i], s1[i]), sm_[i]));
    }
    mx = sw32(sw16(mx, true), true);
    float sum = 0.f, summ = 0.f;
#pragma unroll
    for (int i = 0; i < 4; ++i) {
      s0[i] = __builtin_amdgcn_exp2f(s0[i] - mx); s1[i] = __builtin_amdgcn_exp2f(s1[i] - mx); sm_[i] = __builtin_amdgcn_exp2f(sm_[i] - mx);
      sum += s0[i] + s1[i]; summ += sm_[i];
    }
    const float inv = __builtin_amdgcn_rcpf(sw32(sw16(sum + 0.5f * summ, false), false));
    const u32x4 pf = cat(pk4(s0), pk4(s1));
    const u32x2 pm = pk4(sm_), zz = {0u, 0u}, pmx = g < 2 ? pm : zz, pmy = g < 2 ? zz : pm;
#pragma unroll
    for (int eb = 0; eb < 4; ++eb) {
      const f32x4 ox = mma16(vm[eb], pmx, mma(vf[eb][0], pf, z4)), oy = mma16(vm[eb], pmy, mma(vf[eb][1], pf, z4));
      lw16(sm + (qt * 8 + (w >> 1) * 4 + eb) * 1024 + l * 16, cat(pk4(ox * inv), pk4(oy * inv)));
    }
    bar();
#pragma unroll
    for (int t2 = 0; t2 < 2; ++t2) {
      f32x4 acc[8];
#pragma unroll
      for (int t = 0; t < 8; ++t) acc[t] = z4;
#pragma unroll
      for (int ks = 0; ks < 8; ++ks) {
        const u32x4 of = lr16(sm + (t2 * 8 + ks) * 1024 + l * 16);
#pragma unroll
        for (int t = 0; t < 8; ++t) acc[t] = mma(W[ks][t], of, acc[t]);
      }
#pragma unroll
      for (int u = 0; u < 4; ++u)
        st16(cat(pk4(acc[2 * u]), pk4(acc[2 * u + 1])), rw, hp * 32768 + t2 * 16384 + (w * 4 + u) * 1024 + l * 16, O_P1 + s * 524288);
    }
    waitvm<0>();
    bar();
    if (tid == 0) add_flag(align_ws(p.wsraw), F_P1 + s);
  }
}

DEV void role_norm(const Params& p, rsrc_t rw, LAS char* sm, int set, int j) {
  const int tid = threadIdx.x, l = tid & 63, w = __builtin_amdgcn_readfirstlane(tid >> 6), t4 = tid & 3, pc = tid >> 2;
  const float* mod = (const float*)(align_ws(p.wsraw) + O_MOD) + pc * 8;
  float ga[8], sb[8], cb[8];
#pragma unroll
  for (int i = 0; i < 8; ++i) { ga[i] = mod[1024 + i]; sb[i] = mod[1536 + i]; cb[i] = 1.f + mod[2048 + i]; }
  LAS float* red = (LAS float*)sm;
  const u32 vo = (j >> 2) * 16384 + pc * 256 + ((4 * j + t4) & 15) * 16;
  for (int s = set; s < 64; s += 3) {
    const u32x4 xv = *(const u32x4*)(p.x + (size_t)(s * 32 + 4 * j + t4) * 512 + pc * 8);
    if (!wg_wait(rw, sm, w, F_P1 + s, 16, l < 1)) return;
    u32x4 pp[16];
#pragma unroll
    for (int i = 0; i < 16; ++i) pp[i] = ld16(rw, vo + i * 32768, O_P1 + s * 524288);
    float v[8] = {0, 0, 0, 0, 0, 0, 0, 0};
#pragma unroll
    for (int i = 0; i < 16; ++i)
#pragma unroll
      for (int e = 0; e < 4; ++e) { v[2 * e] += lo(pp[i][e]); v[2 * e + 1] += hi(pp[i][e]); }
    u32x4 x1; float su = 0.f, q = 0.f;
#pragma unroll
    for (int e = 0; e < 4; ++e) {
      x1[e] = pk(lo(xv[e]) + ga[2 * e] * rb(v[2 * e]), hi(xv[e]) + ga[2 * e + 1] * rb(v[2 * e + 1]));
      v[2 * e] = lo(x1[e]); v[2 * e + 1] = hi(x1[e]); su += v[2 * e] + v[2 * e + 1];
    }
    st16(x1, rw, vo, O_X1 + s * 32768);
    su = sw32(sw16(ror_add<8>(ror_add<4>(su)), false), false);
    if (l < 4) red[w * 4 + l] = su;
    bar();
    su = (red[t4] + red[4 + t4] + red[8 + t4] + red[12 + t4]) * (1.f / 512);
#pragma unroll
    for (int e = 0; e < 8; ++e) { v[e] -= su; q += v[e] * v[e]; }
    q = sw32(sw16(ror_add<8>(ror_add<4>(q)), false), false);
    if (l < 4) red[16 + w * 4 + l] = q;
    bar();
    const float rs = rsqrtf((red[16 + t4] + red[20 + t4] + red[24 + t4] + red[28 + t4]) * (1.f / 512) + 1e-5f);
    u32x4 o;
#pragma unroll
    for (int e = 0; e < 4; ++e) o[e] = pk(v[2 * e] * rs * cb[2 * e] + sb[2 * e], v[2 * e + 1] * rs * cb[2 * e + 1] + sb[2 * e + 1]);
    st16(o, rw, vo, O_H2 + s * 32768);
    waitvm<0>();
    bar();
    if (tid == 0) add_flag(align_ws(p.wsraw), F_H2 + s);
  }
}

DEV float gelu(float x) {
  const float u = x + 0.044715f * x * x * x;
  return x * __builtin_amdgcn_rcpf(1.f + __builtin_amdgcn_exp2f(u * (-2.f * 0.7978845608028654f * 1.4426950408889634f)));
}
DEV void role_mlp(const Params& p, rsrc_t rw, LAS char* sm, int set, int fs) {
  const int tid = threadIdx.x, l = tid & 63, w = __builtin_amdgcn_readfirstlane(tid >> 6);
  const int g = l >> 4, q4 = (l >> 2) & 3, p4 = l & 3;
  const f32x4 z4 = {0.f, 0.f, 0.f, 0.f};
  const bool pad = fs == 10 && w == 3;
  const int f0 = pad ? 0 : fs * 128 + w * 32;
  const u32 dst = (u32)(size_t)sm + w * 32768;
  u32x4 W1[16][2], W2[4][8];
  const char* g1 = (const char*)(p.w1 + f0);
  const char* g2 = (const char*)(p.w2 + (size_t)fs * 128 * 512 + w * 128);
  stage_w<256, 64>(g1, 2752, l, dst); stage_w<256, 64>(g1 + 256 * 2752, 2752, l, dst + 16384);
#pragma unroll
  for (int c = 0; c < 2; ++c) {
    waitvm<16>();
    LAS char* a = sm + w * 32768 + c * 16384 + (8 * g + q4) * 64 + p4 * 16;
#pragma unroll
    for (int k = 0; k < 8; ++k)
#pragma unroll
      for (int t = 0; t < 2; ++t) W1[c * 8 + k][t] = cat(trd(a + k * 2048 + t * 8), trd(a + k * 2048 + t * 8 + 256));
    waitlds();
    stage_w<32, 256>(g2 + c * 64 * 1024, 1024, l, dst + c * 16384);
    stage_w<32, 256>(g2 + ((c == 1 && fs == 10) ? 0 : (c * 64 + 32) * 1024), 1024, l, dst + c * 16384 + 8192);
  }
#pragma unroll
  for (int c = 0; c < 2; ++c) {
    if (c == 0) waitvm<16>(); else waitvm<0>();
    LAS char* a = sm + w * 32768 + c * 16384 + (8 * g + q4) * 256 + p4 * 16;
#pragma unroll
    for (int k = 0; k < 2; ++k)
#pragma unroll
      for (int t = 0; t < 8; ++t) {
        LAS char* q = a + k * 8192 + (t >> 1) * 64 + (t & 1) * 8;
        W2[c * 2 + k][t] = cat(trd(q), trd(q + 1024));
      }
  }
  if (fs == 10) {
#pragma unroll
    for (int t = 0; t < 8; ++t) W2[3][t] = u32x4{0u, 0u, 0u, 0u};
  }
  const u32x4 b1v = *(const u32x4*)(p.b1 + f0 + g * 8);
  for (int s = set; s < 64; s += 3) {
    if (!wg_wait(rw, sm, w, F_H2 + s, 8, l < 1)) return;
    u32x4 pf[8];
#pragma unroll
    for (int j = 0; j < 8; ++j) pf[j] = ld16(rw, w * 8192 + j * 1024 + l * 16, O_H2 + s * 32768);
#pragma unroll
    for (int j = 0; j < 8; ++j) lw16(sm + w * 8192 + j * 1024 + l * 16, pf[j]);
    bar();
#pragma unroll
    for (int t2 = 0; t2 < 2; ++t2) {
      f32x4 a0 = {lo(b1v[0]), hi(b1v[0]), lo(b1v[1]), hi(b1v[1])}, a1 = {lo(b1v[2]), hi(b1v[2]), lo(b1v[3]), hi(b1v[3])};
#pragma unroll
      for (int ks = 0; ks < 16; ++ks) {
        const u32x4 hf = lr16(sm + t2 * 16384 + ks * 1024 + l * 16);
        a0 = mma(W1[ks][0], hf, a0); a1 = mma(W1[ks][1], hf, a1);
      }
#pragma unroll
      for (int i = 0; i < 4; ++i) { a0[i] = pad ? 0.f : gelu(rb(a0[i])); a1[i] = pad ? 0.f : gelu(rb(a1[i])); }
      lw16(sm + 32768 + (t2 * 4 + w) * 1024 + l * 16, cat(pk4(a0), pk4(a1)));
    }
    bar();
#pragma unroll
    for (int t2 = 0; t2 < 2; ++t2) {
      f32x4 acc[8];
#pragma unroll
      for (int t = 0; t < 8; ++t) acc[t] = z4;
#pragma unroll
      for (int ks = 0; ks < 4; ++ks) {
        const u32x4 gf = lr16(sm + 32768 + (t2 * 4 + ks) * 1024 + l * 16);
#pragma unroll
        for (int t = 0; t < 8; ++t) acc[t] = mma(W2[ks][t], gf, acc[t]);
      }
#pragma unroll
      for (int u = 0; u < 4; ++u)
        st16(cat(pk4(acc[2 * u]), pk4(acc[2 * u + 1])), rw, fs * 32768 + t2 * 16384 + (w * 4 + u) * 1024 + l * 16, O_P2 + s * 360448);
    }
    waitvm<0>();
    bar();
    if (tid == 0) add_flag(align_ws(p.wsraw), F_P2 + s);
  }
}

DEV void role_out(const Params& p, rsrc_t rw, LAS char* sm, int set, int j) {
  const int tid = threadIdx.x, l = tid & 63, w = __builtin_amdgcn_readfirstlane(tid >> 6), t4 = tid & 3, pc = tid >> 2;
  const float* mod = (const float*)(align_ws(p.wsraw) + O_MOD) + pc * 8;
  float gb[8];
#pragma unroll
  for (int i = 0; i < 8; ++i) gb[i] = mod[2560 + i];
  const u32x4 b2v = *(const u32x4*)(p.b2 + pc * 8);
  const u32 vo = (j >> 2) * 16384 + pc * 256 + ((4 * j + t4) & 15) * 16;
  for (int s = set; s < 64; s += 3) {
    if (!wg_wait(rw, sm, w, F_P2 + s, 11, l < 1)) return;
    u32x4 pp[11];
#pragma unroll
    for (int i = 0; i < 11; ++i) pp[i] = ld16(rw, vo + i * 32768, O_P2 + s * 360448);
    const u32x4 x1 = ld16(rw, vo, O_X1 + s * 32768);
    u32x4 o;
#pragma unroll
    for (int e = 0; e < 4; ++e) {
      float a = 0.f, b = 0.f;
#pragma unroll
      for (int i = 0; i < 11; ++i) { a += lo(pp[i][e]); b += hi(pp[i][e]); }
      o[e] = pk(lo(x1[e]) + gb[2 * e] * rb(rb(a) + lo(b2v[e])), hi(x1[e]) + gb[2 * e + 1] * rb(rb(b) + hi(b2v[e])));
    }
    *(u32x4*)(p.out + (size_t)(s * 32 + 4 * j + t4) * 512 + pc * 8) = o;
  }
}

__global__ __launch_bounds__(256, 1) void main_kernel(Params p) {
  __shared__ __attribute__((aligned(16))) char smem[L_END];
  LAS char* sm = (LAS char*)smem;
  const int b = blockIdx.x, i4 = (b >> 3) * 4 + (b & 3);
  if ((b & 4) == 0 && b < 128) { role_recur(p, sm, i4); return; }
  const int wk = (b & 4) ? i4 : 64 + i4;
  rsrc_t rw = mkrsrc(align_ws(p.wsraw), WS_END);
  if (!phase_qkv(p, rw, sm, wk)) return;
  STAMP(128 + wk);
  if (wk < 48) role_attn(p, rw, sm, wk / 16, wk % 16);
  else if (wk < 81) role_mlp(p, rw, sm, (wk - 48) / 11, (wk - 48) % 11);
  else if (wk < 105) role_norm(p, rw, sm, (wk - 81) / 8, (wk - 81) % 8);
  else if (wk >= 128 && wk < 152) role_out(p, rw, sm, (wk - 128) / 8, (wk - 128) % 8);
  STAMP(384 + wk);
}

extern "C" void kernel_launch(void* const* d_in, const int* in_sizes, int n_in, void* d_out, int out_size, void* d_ws,
                              size_t ws_size, hipStream_t stream) {
  Params p;
  p.x = (const __bf16*)d_in[0]; p.mem0 = (const __bf16*)d_in[1]; p.wq = (const __bf16*)d_in[2]; p.wk = (const __bf16*)d_in[3];
  p.wv = (const __bf16*)d_in[4]; p.wo = (const __bf16*)d_in[5]; p.w1 = (const __bf16*)d_in[8]; p.b1 = (const __bf16*)d_in[9];
  p.w2 = (const __bf16*)d_in[10]; p.b2 = (const __bf16*)d_in[11]; p.c = (const __bf16*)d_in[12]; p.wmod = (const __bf16*)d_in[13];
  p.out = (__bf16*)d_out;
  p.wsraw = (char*)d_ws;
  prologue_kernel<<<256, 256, 0, stream>>>(p);
  main_kernel<<<256, 256, 0, stream>>>(p);
}
```

```cpp
#include <hip/hip_runtime.h>
#include <hip/hip_bf16.h>

typedef unsigned u32;
typedef unsigned u32x2 __attribute__((ext_vector_type(2)));
typedef unsigned u32x4 __attribute__((ext_vector_type(4)));
typedef unsigned u32x8 __attribute__((ext_vector_type(8)));
typedef float f32x2 __attribute__((ext_vector_type(2)));
typedef float f32x4 __attribute__((ext_vector_type(4)));
typedef short s16x4 __attribute__((ext_vector_type(4)));
typedef __bf16 bf2 __attribute__((ext_vector_type(2)));
typedef __bf16 bf8 __attribute__((ext_vector_type(8)));
typedef __bf16 bf16v __attribute__((ext_vector_type(16)));
typedef __amdgpu_buffer_rsrc_t rsrc_t;
#define LAS __attribute__((address_space(3)))
#define DEV __device__ __forceinline__
constexpr int SC1 = 16;
constexpr u32 SPIN_CAP = 400000;

constexpr u32 MB = 1u << 20;
constexpr u32 O_MEM = 0;
constexpr u32 O_RING = 1 * MB;
constexpr u32 O_CTRL = 2 * MB;
constexpr u32 O_MOD = 2 * MB + 131072;
constexpr u32 O_H = 4 * MB, O_X1 = 6 * MB, O_H2 = 8 * MB, O_KM = 10 * MB, O_VM = 14 * MB;
constexpr u32 O_Q = 18 * MB, O_K = 34 * MB, O_V = 50 * MB, O_P1 = 66 * MB, O_P2 = 98 * MB, WS_END = 120 * MB;
constexpr u32 F_TMO = 0, F_SIDE = 1, F_H = 2, F_KV = F_H + 64, F_KM = F_KV + 192, F_P1 = F_KM + 64, F_H2 = F_P1 + 64,
              F_P2 = F_H2 + 64, F_END = F_P2 + 64;

struct Params {
  const __bf16 *x, *mem0, *wq, *wk, *wv, *wo, *w1, *b1, *w2, *b2, *c, *wmod;
  __bf16* out;
  char* wsraw;
};

DEV char* align_ws(char* p) { return p + ((0 - (size_t)p) & (2 * MB - 1)); }
DEV rsrc_t mkrsrc(const void* p, u32 n) { return __builtin_amdgcn_make_buffer_rsrc((void*)p, 0, n, 0x00020000); }
DEV u32x4 ld16(rsrc_t r, u32 v, u32 s) { return __builtin_amdgcn_raw_buffer_load_b128(r, v, s, SC1); }
DEV u32x2 ld8(rsrc_t r, u32 v, u32 s) { return __builtin_amdgcn_raw_buffer_load_b64(r, v, s, SC1); }
DEV u32 ld4(rsrc_t r, u32 v, u32 s) { return __builtin_amdgcn_raw_buffer_load_b32(r, v, s, SC1); }
DEV void st16(u32x4 d, rsrc_t r, u32 v, u32 s) { __builtin_amdgcn_raw_buffer_store_b128(d, r, v, s, SC1); }
DEV void st8(u32x2 d, rsrc_t r, u32 v, u32 s) { __builtin_amdgcn_raw_buffer_store_b64(d, r, v, s, SC1); }
DEV void st4(u32 d, rsrc_t r, u32 v, u32 s) { __builtin_amdgcn_raw_buffer_store_b32(d, r, v, s, SC1); }
DEV u32 pk(float a, float b) { f32x2 v = {a, b}; return __builtin_bit_cast(u32, __builtin_convertvector(v, bf2)); }
DEV float lo(u32 v) { return __uint_as_float(v << 16); }
DEV float hi(u32 v) { return __uint_as_float(v & 0xffff0000u); }
DEV float rb(float a) { return lo(pk(a, a)); }
DEV u32x2 trd(LAS char* p) { return __builtin_bit_cast(u32x2, __builtin_amdgcn_ds_read_tr16_b64_v4i16((LAS s16x4*)p)); }
DEV u32x4 lr16(LAS char* p) { return *(LAS u32x4*)p; }
DEV void lw16(LAS char* p, u32x4 v) { *(LAS u32x4*)p = v; }
DEV void lw8(LAS char* p, u32x2 v) { *(LAS u32x2*)p = v; }
DEV u32x4 cat(u32x2 a, u32x2 b) { return u32x4{a.x, a.y, b.x, b.y}; }
DEV u32x8 cat(u32x4 a, u32x4 b) { return u32x8{a.x, a.y, a.z, a.w, b.x, b.y, b.z, b.w}; }
DEV f32x4 mma(u32x4 a, u32x4 b, f32x4 c) {
  return __builtin_amdgcn_mfma_f32_16x16x32_bf16(__builtin_bit_cast(bf8, a), __builtin_bit_cast(bf8, b), c, 0, 0, 0);
}
DEV f32x4 mma16(u32x2 a, u32x2 b, f32x4 c) {
  return __builtin_amdgcn_mfma_f32_16x16x16bf16_1k(__builtin_bit_cast(s16x4, a), __builtin_bit_cast(s16x4, b), c, 0, 0, 0);
}
DEV f32x4 smm(u32x4 a, u32x8 b, f32x4 c, u32 idx) {
  return __builtin_amdgcn_smfmac_f32_16x16x64_bf16(__builtin_bit_cast(bf8, a), __builtin_bit_cast(bf16v, b), c, idx, 0, 0);
}
#define MEMB asm volatile("" ::: "memory")
DEV float max3(float a, float b, float c) { float r; asm("v_max3_f32 %0, %1, %2, %3" : "=v"(r) : "v"(a), "v"(b), "v"(c)); return r; }
DEV void bar() { asm volatile("s_waitcnt lgkmcnt(0)\n\ts_barrier" ::: "memory"); }
template <int N> DEV void waitvm() { asm volatile("s_waitcnt vmcnt(%0)" ::"i"(N) : "memory"); }
DEV void waitlds() { asm volatile("s_waitcnt lgkmcnt(0)" ::: "memory"); }
DEV void dma16(const void* g, u32 ldsaddr) {
  u32 keep;
  asm volatile("s_mov_b32 %0, m0\n\ts_mov_b32 m0, %2\n\ts_nop 0\n\tglobal_load_lds_dwordx4 %1, off\n\ts_mov_b32 m0, %0"
               : "=&s"(keep) : "v"(g), "s"(ldsaddr) : "memory");
}
DEV float sw16(float a, bool mx) {
  auto r = __builtin_amdgcn_permlane16_swap(__float_as_uint(a), __float_as_uint(a), false, false);
  float x = __uint_as_float(r[0]), y = __uint_as_float(r[1]);
  return mx ? fmaxf(x, y) : x + y;
}
DEV float sw32(float a, bool mx) {
  auto r = __builtin_amdgcn_permlane32_swap(__float_as_uint(a), __float_as_uint(a), false, false);
  float x = __uint_as_float(r[0]), y = __uint_as_float(r[1]);
  return mx ? fmaxf(x, y) : x + y;
}
template <int C> DEV float ror_add(float u) {
  return u + __int_as_float(__builtin_amdgcn_update_dpp(0, __float_as_int(u), 0x120 + C, 0xf, 0xf, true));
}
DEV f32x4 fold(f32x4 x, f32x4 y) {
  f32x4 o;
#pragma unroll
  for (int i = 0; i < 4; ++i) {
    auto r = __builtin_amdgcn_permlane32_swap(__float_as_uint(x[i]), __float_as_uint(y[i]), false, false);
    o[i] = __uint_as_float(r[0]) + __uint_as_float(r[1]);
  }
  return o;
}
DEV f32x4 vzero() { f32x4 z; asm volatile("v_mov_b32 %0, 0" : "=v"(z[0])); asm volatile("v_mov_b32 %0, 0" : "=v"(z[1])); asm volatile("v_mov_b32 %0, 0" : "=v"(z[2])); asm volatile("v_mov_b32 %0, 0" : "=v"(z[3])); return z; }
DEV u32x2 pk4(f32x4 v) { return u32x2{pk(v[0], v[1]), pk(v[2], v[3])}; }
DEV u32 nosent(u32 v) { return v < 0xfffffffeu ? v : 0xfffffffeu; }
DEV bool valid(u32x4 a) { return a.x != ~0u && a.y != ~0u && a.z != ~0u && a.w != ~0u; }
DEV u32 nearoff(u32 off, u32 side) { return ((off >> 13) << 14) | (off & 8191) | (side << 13); }
DEV u32 memoff(u32 k, u32 quad) {
  u32 hb = (k >> 4) & 1;
  return (k >> 6) * 1024 + hb * 512 + quad * 256 + ((k & 15) ^ (8 * quad)) * 16 + (((k >> 5) & 1) ^ hb) * 8;
}
template <int N> DEV void delay() {
#pragma unroll
  for (int i = 0; i < N; ++i) asm volatile("s_nop 7" ::: "memory");
}
DEV u32 umax3(u32 a, u32 b, u32 c) { u32 r; asm("v_max3_u32 %0, %1, %2, %3" : "=v"(r) : "v"(a), "v"(b), "v"(c)); return r; }
DEV float fadd(float a, float b) { float r; asm("v_add_f32 %0, %1, %2" : "=v"(r) : "v"(a), "v"(b)); return r; }
DEV float fmul(float a, float b) { float r; asm("v_mul_f32 %0, %1, %2" : "=v"(r) : "v"(a), "v"(b)); return r; }
DEV f32x4 add4(f32x4 a, f32x4 b) { return f32x4{fadd(a[0], b[0]), fadd(a[1], b[1]), fadd(a[2], b[2]), fadd(a[3], b[3])}; }
DEV f32x4 mul4(f32x4 a, float b) { return f32x4{fmul(a[0], b), fmul(a[1], b), fmul(a[2], b), fmul(a[3], b)}; }
DEV void halt() { asm volatile("s_endpgm" ::: "memory"); __builtin_unreachable(); }
DEV bool nosentinel(u32 m) { return __builtin_amdgcn_ballot_w64(m == ~0u) == 0; }
#define SLP 8
DEV u32 wait_flags(rsrc_t rw, u32 flag, u32 target, bool active) {
  for (u32 spins = 0;; ++spins) {
    asm volatile("" ::: "memory");
    u32 v = ld4(rw, O_CTRL + (active ? flag : F_TMO) * 128, 0);
    if (__all(!active || v >= target)) return __builtin_amdgcn_readfirstlane(v);
    if (spins > SPIN_CAP || __any(!active && v != 0)) { st4(1, rw, O_CTRL + F_TMO * 128, 0); return 0; }
    __builtin_amdgcn_s_sleep(SLP);
  }
}
#define STAMP(i)
#define PROFV(i, v)
#define CYC(i)
DEV void add_flag(char* ws, u32 flag) {
  __hip_atomic_fetch_add((__attribute__((address_space(1))) u32*)(ws + O_CTRL + flag * 128), 1u, __ATOMIC_RELAXED, __HIP_MEMORY_SCOPE_AGENT);
}

#define NPROBE 3
__global__ __launch_bounds__(256) void prologue_kernel(Params p) {
  __shared__ float red[4][32];
  const int tid = threadIdx.x, b = blockIdx.x, l = tid & 63, w = tid >> 6;
  if (b == 0) STAMP(0);
  rsrc_t rw = mkrsrc(align_ws(p.wsraw), WS_END);
  for (u32 j = b * 256 + tid; j < 2 * MB / 16; j += 65536) {
    u32x4 v = {~0u, ~0u, ~0u, ~0u};
    if (j < 1024) {
      u32 ch = j & 511, hb = (ch >> 5) & 1, quad = (ch >> 4) & 1, kk = (ch >> 6) * 64 + hb * 16 + ((ch & 15) ^ (8 * quad));
      const unsigned short* m = (const unsigned short*)p.mem0 + quad * 4 * 512;
      u32 ka = kk + hb * 32, kb = kk + (hb ^ 1) * 32;
      v = u32x4{nosent(m[ka] | (u32)m[512 + ka] << 16), nosent(m[1024 + ka] | (u32)m[1536 + ka] << 16),
                nosent(m[kb] | (u32)m[512 + kb] << 16), nosent(m[1024 + kb] | (u32)m[1536 + kb] << 16)};
    }
    st16(v, rw, j * 16, 0);
  }
  if (b >= 96 && b < 99) { u32 f = (b - 96) * 256 + tid; if (f < F_END) st4(0, rw, O_CTRL + f * 128, 0); }
  if (b < 96) {
    const int kq = tid >> 2, cq = tid & 3;
    u32x4 cv = *(const u32x4*)(p.c + kq * 8);
    float acc[8] = {0, 0, 0, 0, 0, 0, 0, 0};
#pragma unroll
    for (int i = 0; i < 8; ++i) {
      float cc = (i & 1) ? hi(cv[i >> 1]) : lo(cv[i >> 1]);
      float s = rb(cc / (1.f + __expf(-cc)));
      u32x4 wv = *(const u32x4*)(p.wmod + (size_t)(kq * 8 + i) * 3072 + b * 32 + cq * 8);
#pragma unroll
      for (int j = 0; j < 4; ++j) { acc[2 * j] += s * lo(wv[j]); acc[2 * j + 1] += s * hi(wv[j]); }
    }
#pragma unroll
    for (int j = 0; j < 8; ++j) { float t = sw32(sw16(ror_add<8>(ror_add<4>(acc[j])), false), false); if (l < 4) red[w][l * 8 + j] = t; }
    __syncthreads();
    if (tid < 32) ((float*)(align_ws(p.wsraw) + O_MOD))[b * 32 + tid] = rb(red[0][tid] + red[1][tid] + red[2][tid] + red[3][tid]);
  }
}

constexpr u32 L_MEM = 131072, L_K = L_MEM + 8192, L_Q = L_K + 8192, L_KM = L_Q + 2048, L_O = L_KM + 2048, L_V = L_O + 4096, L_END = L_V + 8192;
#define D1 7
#define D2 18

DEV void stage_qkv(const Params& p, int i, int h, int w, int l, u32 smb) {
  const __bf16* W = i < 4 ? p.wq : (i < 8 ? p.wv : p.wk);
#pragma unroll
  for (int j = 0; j < 8; ++j) {
    const int row = w * 32 + j * 4 + (l >> 4), sw = 2 * ((l >> 4) | ((j >> 1) & 1) << 2);
    dma16((const char*)(W + (size_t)((i & 3) * 128 + row) * 4096 + h * 128) + ((l & 15) ^ sw) * 16, smb + (i & 3) * 32768 + w * 8192 + j * 1024);
  }
}
DEV void read_qkv(LAS char* src, int w, int l, u32x8 (&W)[8][2], int B0) {
  const int g = l >> 4, q4 = (l >> 2) & 3, p4 = l & 3, sw = 2 * (q4 | (g & 1) << 2);
#pragma unroll
  for (int t = 0; t < 2; ++t) {
    LAS char* a = src + (g * 8 + q4) * 256 + ((w * 4 + t * 2 + (p4 >> 1)) ^ sw) * 16 + (p4 & 1) * 8;
#pragma unroll
    for (int B = 0; B < 2; ++B) {
      LAS char* q = a + B * 16384;
      W[B0 + B][t] = cat(cat(trd(q), trd(q + 1024)), cat(trd(q + 8192), trd(q + 9216)));
    }
  }
}

DEV void role_recur(const Params& p, LAS char* sm, int r) {
  const int tid = threadIdx.x, l = tid & 63, w = __builtin_amdgcn_readfirstlane(tid >> 6);
  const int h = (r & 3) + 4 * (r >> 3), c = (r >> 2) & 1, rr = h * 2 + c;
  const int g = l >> 4, n = l & 15, q4 = (l >> 2) & 3, p4 = l & 3;
  rsrc_t rw = mkrsrc(align_ws(p.wsraw), WS_END);
  if (r == 0) {
    const char* q = align_ws(p.wsraw) + O_MEM + 4096 + w * 1024;
    u32 best[2] = {~0u, ~0u}, d;
    asm volatile("global_load_dword %0, %1, off sc1\n\ts_waitcnt vmcnt(0)" : "=v"(d) : "v"(q - 4096) : "memory");
#pragma unroll 1
    for (int i = 0; i < NPROBE; ++i)
#pragma unroll
      for (int e = 0; e < 2; ++e) {
        unsigned long long t1, t2;
        asm volatile("s_memtime %0\n\ts_waitcnt lgkmcnt(0)\n\tglobal_load_dword %2, %3, off sc1\n\ts_waitcnt vmcnt(0)\n\ts_memtime %1\n\ts_waitcnt lgkmcnt(0)"
                     : "=&s"(t1), "=&s"(t2), "=&v"(d) : "v"(q + e * 8192 + i * 256) : "memory");
        best[e] = min(best[e], (u32)(t2 - t1));
      }
    LAS u32* lat = (LAS u32*)(sm + L_MEM);
    lat[w * 2] = best[0]; lat[w * 2 + 1] = best[1];
    bar();
    const u32 l0 = min(min(lat[0], lat[2]), min(lat[4], lat[6])), l1 = min(min(lat[1], lat[3]), min(lat[5], lat[7]));
    if (tid == 0) st4(l0 <= l1 ? 1u : 2u, rw, O_CTRL + F_SIDE * 128, 0);
    PROFV(11, l0 * 10000 + l1);
    STAMP(12);
  }
  const u32 smb = (u32)(size_t)sm;
  LAS char* stA = sm + w * 32768; LAS char* stB = stA + 16384;
  if (r == 0) STAMP(1);
  u32 np1 = 0, np2 = 0;
  u32x8 Wq[8][2], Wv[8][2], Wk[8][2];
  stage_qkv(p, 0, h, w, l, smb); stage_qkv(p, 1, h, w, l, smb); stage_qkv(p, 2, h, w, l, smb);
#pragma unroll
  for (int i = 0; i < 12; ++i) {
    if (i == 10) waitvm<8>(); else waitvm<16>();
    bar();
    if (i < 9) stage_qkv(p, i + 3, h, w, l, smb);
    if (i == 10) {
      const char* gp = (const char*)(p.wo + (size_t)(h * 128 + (l >> 3)) * 512 + c * 256 + w * 64) + (l & 7) * 16;
#pragma unroll
      for (int j = 0; j < 16; ++j) dma16(gp + j * 8 * 1024, smb + w * 16384 + j * 1024);
    }
    read_qkv(sm + (i & 3) * 32768, w, l, i < 4 ? Wq : (i < 8 ? Wv : Wk), (i & 3) * 2);
    waitlds();
  }
  {
    u32x8 T[4][2];
    waitvm<0>();
    LAS char* a = sm + w * 16384 + (16 * (g & 1) + 4 * (g >> 1) + (q4 & 1)) * 128 + (q4 >> 1) * 32 + p4 * 8;
#pragma unroll
    for (int eb = 0; eb < 4; ++eb)
#pragma unroll
      for (int t = 0; t < 2; ++t) {
        LAS char* q = a + eb * 4096 + t * 64;
        T[eb][t] = cat(cat(trd(q), trd(q + 256)), cat(trd(q + 1024), trd(q + 1280)));
      }
    bar();
#pragma unroll
    for (int B = 0; B < 4; ++B)
#pragma unroll
      for (int t = 0; t < 2; ++t) {
        const u32x8 x = T[B][t], y = Wk[4 + B][t];
        lw16(stA + (B * 4 + t * 2) * 1024 + l * 16, u32x4{x[0], x[1], x[2], x[3]});
        lw16(stA + (B * 4 + t * 2 + 1) * 1024 + l * 16, u32x4{x[4], x[5], x[6], x[7]});
        lw16(stB + (B * 4 + t * 2) * 1024 + l * 16, u32x4{y[0], y[1], y[2], y[3]});
        lw16(stB + (B * 4 + t * 2 + 1) * 1024 + l * 16, u32x4{y[4], y[5], y[6], y[7]});
      }
  }
  u32 side = wait_flags(rw, F_SIDE, 1, l == 0);
  if (!side) halt();
  side = (side - 1) & 1;
  const u32 idx = (l & 8) ? 0xEEEEu : 0x4444u;
  u32x4 sel;
#pragma unroll
  for (int j = 0; j < 4; ++j) sel[j] = ((l & 7) == 2 * j ? 0x3f80u : 0u) | ((l & 7) == 2 * j + 1 ? 0x3f800000u : 0u);
  LAS char* aA0 = sm + L_MEM + memoff(16 * g + 4 * (q4 >> 1) + (q4 & 1) + 2 * (p4 >> 1), p4 & 1);
  LAS char* aA1 = sm + L_MEM + memoff(16 * g + 8 + 4 * (q4 >> 1) + (q4 & 1) + 2 * (p4 >> 1), p4 & 1);
  LAS char* aXw = sm + L_Q + w * 512 + (g & 1) * 256 + ((16 * (g >> 1) + n) ^ (4 * (g & 1))) * 8;
  LAS char* aX0 = sm + L_Q + (p4 & 1) * 256 + ((8 * g + q4) ^ (4 * (p4 & 1))) * 8;
  LAS char* aX1 = sm + L_Q + (p4 & 1) * 256 + ((8 * g + 4 + q4) ^ (4 * (p4 & 1))) * 8;
  const u32 vMem = side * 8192 + w * 2048 + l * 16;
  const u32 vPst = nearoff((c * 32 + w * 8 + (l >> 3)) * 4096 + h * 128 + (l & 7) * 16, side);
  const u32 vPld = nearoff(rr * 4096 + (l >> 1) * 128 + w * 32 + (l & 1) * 16, side);
  u32 vMst;
  {
    u32 ch = rr * 8 + w * 2 + (l & 1), gg = (ch >> 4) & 3;
    vMst = side * 8192 + memoff((ch >> 6) * 64 + 32 * (g & 1) + 16 * (gg >> 1) + (ch & 15), gg & 1);
  }
  const u32 vKV = h * 8192 + w * 2048 + l * 16, vKV2 = vKV + 1024;
  if (r == 0) STAMP(2);
  STAMP(600 + r);
  u32 prog = wait_flags(rw, F_KV + h, 1, l == 0);
  if (!prog) halt();
  STAMP(664 + r);
  u32x4 kn0 = ld16(rw, vKV, O_K), kn1 = ld16(rw, vKV + 1024, O_K), vn0 = ld16(rw, vKV, O_V), vn1 = ld16(rw, vKV + 1024, O_V);
  const f32x4 z4 = {0.f, 0.f, 0.f, 0.f};
  const u32x4 ones = {0x3f803f80u, 0x3f803f80u, 0x3f803f80u, 0x3f803f80u};
  const u32x2 ones2 = {0x3f803f80u, 0x3f803f80u};
  const float sc = 0.08838834764831845f * 1.4426950408889634f;

  LAS char* aWo = stA + l * 16; LAS char* aWk = stB + l * 16;
  LAS char* aK = sm + L_K + l * 16; LAS char* aO = sm + L_O + l * 16; LAS char* aM = sm + L_MEM + w * 2048 + l * 16;
  LAS char* aV = sm + L_V + w * 2048 + l * 16;
#define PIN(x) asm volatile("" : "+v"(x))
#define PIN2(x, y) asm volatile("" : "+v"(x), "+v"(y))
#define NOPD(n, x, y) asm volatile("s_nop " #n : "+v"(x), "+v"(y))
#define SMA(d, a, b) asm volatile("v_smfmac_f32_16x16x64_bf16 %0, %1, %2, %3" : "+v"(d) : "v"(a), "a"(b), "v"(idx))
#define SMV(d, a, b) asm volatile("v_smfmac_f32_16x16x64_bf16 %0, %1, %2, %3" : "+v"(d) : "v"(a), "v"(b), "v"(idx))
#define MM0(d, a, b) asm volatile("v_mfma_f32_16x16x32_bf16 %0, %1, %2, 0" : "=&v"(d) : "v"(a), "v"(b))
#define MMA(d, a, b) asm volatile("v_mfma_f32_16x16x32_bf16 %0, %1, %2, %0" : "+v"(d) : "v"(a), "v"(b))
#define M16(d, a, b) asm volatile("v_mfma_f32_16x16x16_bf16 %0, %1, %2, %0" : "+v"(d) : "v"(a), "v"(b))
#define SWP(x, y, i) do { auto r_ = __builtin_amdgcn_permlane32_swap(__float_as_uint(x[i]), __float_as_uint(y[i]), false, false); \
    x[i] = __uint_as_float(r_[0]); y[i] = __uint_as_float(r_[1]); } while (0)
#define LW(a, i) cat(lr16((a) + (i) * 2048), lr16((a) + (i) * 2048 + 1024))
#define PUBLISH(s) do { if (c == 0) { const u32x4 kmw = cat(trd(aX0 + 2048 + w * 512), trd(aX1 + 2048 + w * 512)); \
      if (!(l & 8)) st16(kmw, rw, h * 2048 + w * 512 + g * 128 + (l & 7) * 16, O_KM + (s) * 65536); \
    } else st8(vm, rw, h * 2048 + w * 512 + l * 8, O_VM + (s) * 65536); } while (0)
#define LDA(d, v, so) asm volatile("buffer_load_dwordx4 %0, %1, %2, %3 offen sc1" : "=&v"(d) : "v"(v), "s"(rw), "s"(so) : "memory")
#define EX(x) __builtin_amdgcn_exp2f(__builtin_fmaf(x, sc, mx))
  lw16(aK + w * 2048, kn0); lw16(aK + w * 2048 + 1024, kn1);
  lw16(aV, vn0); lw16(aV + 1024, vn1);
  u32x2 vm = {0u, 0u};
  const u32 vPub = h * 2048 + w * 512 + (c ? l * 8 : g * 128 + (l & 7) * 16);
  for (int s = 0;; ++s) {
    f32x4 qx = z4, qy = z4, kx = z4, ky = z4;
    PIN2(qx, qy); PIN2(kx, ky);
    Wk[4][0] = LW(aWk, 0); Wk[4][1] = LW(aWk, 1); Wk[5][0] = LW(aWk, 2); Wk[5][1] = LW(aWk, 3);
    delay<D2>();
    CYC(0);
    bool ok;
    u32 spins = 0;
#pragma unroll 1
    do {
      MEMB;
      const u32x4 ma = ld16(rw, vMem, O_MEM + s * 16384), mb = ld16(rw, vMem + 1024, O_MEM + s * 16384);
      np2 += s >= 20;
      lw16(aM, ma); lw16(aM + 1024, mb);
      ok = nosentinel(umax3(umax3(ma.x, ma.y, ma.z), umax3(mb.x, mb.y, mb.z), umax3(ma.w, mb.w, mb.w)));
    } while (!ok && ++spins < SPIN_CAP);
    if (!ok) { st4(2, rw, O_CTRL + F_TMO * 128, 0); halt(); }
    CYC(1);
    bar();
    CYC(2);
    u32x4 af[8];
#define AF(B) do { MEMB; af[B] = cat(trd(aA0 + (B) * 1024), trd(aA1 + (B) * 1024)); MEMB; } while (0)
    AF(0); AF(1); AF(2); AF(3);
    SMA(qx, af[0], Wq[0][0]); AF(4); SMA(qy, af[0], Wq[0][1]); AF(5);
    SMA(qx, af[1], Wq[1][0]); AF(6); SMA(qy, af[1], Wq[1][1]); AF(7);
    const u32x4 kmp = cat(trd(aX0 + 2048 + w * 512), trd(aX1 + 2048 + w * 512)); MEMB;
    SMA(qx, af[2], Wq[2][0]); MEMB;
    const u32 s1 = s < 63 ? s + 1 : 63;
    if (prog < s1 + 1) {
      prog = wait_flags(rw, F_KV + (s1 >> 5) * 48 + h, (s1 & 31) + 1, l == 0);
      if (!prog) halt();
      prog += s1 & 32;
    }
    LDA(kn0, vKV, O_K + s1 * 262144);
    SMA(qy, af[2], Wq[2][1]);
#pragma unroll
    for (int B = 3; B < 8; ++B) {
      SMA(qx, af[B], Wq[B][0]);
      if (B == 3 && s) { if (c) st8(vm, rw, vPub, O_VM + (s - 1) * 65536); }
      if (B == 4) LDA(kn1, vKV2, O_K + s1 * 262144);
      if (B == 5 && s) { if (!c && !(l & 8)) st16(kmp, rw, vPub, O_KM + (s - 1) * 65536); }
      if (B == 7 && s) { if (tid == 0) st4(s - 1, rw, O_CTRL + (F_KM + rr) * 128, 0); }
      if (B == 6) LDA(vn0, vKV, O_V + s1 * 262144);
      SMA(qy, af[B], Wq[B][1]);
      if (B < 7) { MEMB; Wk[6 + ((B - 3) >> 1)][(B - 3) & 1] = LW(aWk, B + 1); MEMB; }
    }
#pragma unroll
    for (int B = 4; B < 7; ++B) {
      SMV(kx, af[B], Wk[B][0]);
      if (B == 4) LDA(vn1, vKV2, O_V + s1 * 262144);
      SMV(ky, af[B], Wk[B][1]);
    }
    NOPD(1, qx, qy);
    SWP(qx, qy, 0); SWP(qx, qy, 1); PIN2(qx, qy);
    SMV(kx, af[7], Wk[7][0]);
    SWP(qx, qy, 2); SWP(qx, qy, 3); PIN2(qx, qy);
    SMV(ky, af[7], Wk[7][1]);
    qx = add4(qx, qy); PIN(qx);
    SMV(kx, af[0], Wk[0][0]);
    { u32x2 t2 = pk4(qx); PIN(t2); SMV(ky, af[0], Wk[0][1]); MEMB; lw8(aXw, t2); MEMB; }
    u32x4 qf[4], kf[4][2], km[4];
    f32x4 s0, s1v, sma, vx = z4, vy = z4;
    PIN2(vx, vy);
    kf[0][0] = lr16(aK); kf[0][1] = lr16(aK + 1024); MEMB;
    SMV(kx, af[1], Wk[1][0]); MEMB;
    SMV(ky, af[1], Wk[1][1]); MEMB;
    kf[1][0] = lr16(aK + 2048); kf[1][1] = lr16(aK + 3072); MEMB;
    SMV(kx, af[2], Wk[2][0]); MEMB;
    kf[2][0] = lr16(aK + 4096); kf[2][1] = lr16(aK + 5120); MEMB;
    SMV(ky, af[2], Wk[2][1]); MEMB;
    kf[3][0] = lr16(aK + 6144); kf[3][1] = lr16(aK + 7168); MEMB;
    SMV(kx, af[3], Wk[3][0]); SMV(ky, af[3], Wk[3][1]);
    SMA(vx, af[0], Wv[0][0]); SMA(vy, af[0], Wv[0][1]);
    NOPD(0, kx, ky);
    SWP(kx, ky, 0); SWP(kx, ky, 1); PIN2(kx, ky);
    SMA(vx, af[1], Wv[1][0]);
    SWP(kx, ky, 2); SWP(kx, ky, 3); PIN2(kx, ky);
    SMA(vy, af[1], Wv[1][1]);
    kx = add4(kx, ky);
    { u32x2 t2 = pk4(kx); PIN(t2); SMA(vx, af[2], Wv[2][0]); MEMB; lw8(aXw + 2048, t2); MEMB; }
    SMA(vy, af[2], Wv[2][1]); MEMB;
    SMA(vx, af[3], Wv[3][0]); SMA(vy, af[3], Wv[3][1]);
    bar();
    CYC(3);
    qf[0] = cat(trd(aX0), trd(aX1)); km[0] = cat(trd(aX0 + 2048), trd(aX1 + 2048)); MEMB;
    SMA(vx, af[4], Wv[4][0]); MEMB;
    qf[1] = cat(trd(aX0 + 512), trd(aX1 + 512)); km[1] = cat(trd(aX0 + 2560), trd(aX1 + 2560)); MEMB;
    SMA(vy, af[4], Wv[4][1]); MEMB;
    qf[2] = cat(trd(aX0 + 1024), trd(aX1 + 1024)); km[2] = cat(trd(aX0 + 3072), trd(aX1 + 3072)); MEMB;
    SMA(vx, af[5], Wv[5][0]); MEMB;
    qf[3] = cat(trd(aX0 + 1536), trd(aX1 + 1536)); km[3] = cat(trd(aX0 + 3584), trd(aX1 + 3584)); MEMB;
    SMA(vy, af[5], Wv[5][1]); MEMB;
    const u32x4 vc0 = lr16(aV), vc1 = lr16(aV + 1024); MEMB;
    SMA(vx, af[6], Wv[6][0]); SMA(vy, af[6], Wv[6][1]);
    MM0(s0, kf[0][0], qf[0]); MM0(s1v, kf[0][1], qf[0]); MM0(sma, km[0], qf[0]);
#pragma unroll
    for (int eb = 1; eb < 4; ++eb) { MMA(s0, kf[eb][0], qf[eb]); MMA(s1v, kf[eb][1], qf[eb]); MMA(sma, km[eb], qf[eb]); }
    SMA(vx, af[7], Wv[7][0]); MEMB;
    u32x8 Wo[4][2];
    Wo[0][0] = LW(aWo, 0); Wo[0][1] = LW(aWo, 1); MEMB;
    PIN2(s0, s1v);
    float mx = max3(max3(s0[0], s0[1], s0[2]), max3(s0[3], s1v[0], s1v[1]), max3(s1v[2], s1v[3], s1v[3]));
    PIN(mx);
    SMA(vy, af[7], Wv[7][1]); MEMB;
    Wo[1][0] = LW(aWo, 2); Wo[1][1] = LW(aWo, 3); MEMB;
    PIN(sma);
    mx = max3(max3(mx, sma[0], sma[1]), sma[2], sma[3]);
    { auto r_ = __builtin_amdgcn_permlane16_swap(__float_as_uint(mx), __float_as_uint(mx), false, false); mx = max3(__uint_as_float(r_[0]), __uint_as_float(r_[1]), __uint_as_float(r_[1])); }
    { auto r_ = __builtin_amdgcn_permlane32_swap(__float_as_uint(mx), __float_as_uint(mx), false, false); mx = max3(__uint_as_float(r_[0]), __uint_as_float(r_[1]), __uint_as_float(r_[1])) * -sc; }
    asm volatile("" : "+v"(mx), "+v"(vx), "+v"(vy));
#pragma unroll
    for (int i = 0; i < 4; ++i) {
      s0[i] = EX(s0[i]); s1v[i] = EX(s1v[i]);
      SWP(vx, vy, i); vx[i] = fadd(vx[i], vy[i]);
    }
    u32x4 pf = cat(pk4(s0), pk4(s1v));
    vm = pk4(vx);
    asm volatile("s_nop 0" : "+v"(pf), "+v"(vm));
    f32x4 ox, oy, su;
    MM0(su, ones, pf);
    sma[0] = EX(sma[0]); sma[1] = EX(sma[1]); PIN(sma);
    MM0(ox, vc0, pf);
    sma[2] = EX(sma[2]); sma[3] = EX(sma[3]); PIN(sma);
    MM0(oy, vc1, pf);
    u32x2 pm = pk4(sma), pmx, pmy;
    pmx.x = g < 2 ? pm.x : 0u; pmx.y = g < 2 ? pm.y : 0u; pmy.x = g < 2 ? 0u : pm.x; pmy.y = g < 2 ? 0u : pm.y;
    asm volatile("s_nop 0" : "+v"(pmx), "+v"(pmy));
    M16(su, ones2, pmx); M16(ox, vm, pmx); M16(oy, vm, pmy); MEMB;
    Wo[2][0] = LW(aWo, 4); Wo[2][1] = LW(aWo, 5); Wo[3][0] = LW(aWo, 6); Wo[3][1] = LW(aWo, 7);
    asm volatile("" : "+v"(su) :: "memory");
    float inv = __builtin_amdgcn_rcpf(su[0]);
    PIN(inv); PIN2(ox, oy);
    CYC(4);
    if (s == 63) { asm volatile("s_waitcnt vmcnt(0)" :: "v"(kn0), "v"(kn1), "v"(vn0), "v"(vn1) : "memory"); PUBLISH(63); break; }
    {
      lw16(aO + w * 1024, cat(pk4(mul4(ox, inv)), pk4(mul4(oy, inv)))); MEMB;
      f32x4 d0 = z4, d1 = z4;
      PIN2(d0, d1);
      bar();
      CYC(5);
      u32x4 of[4];
#pragma unroll
      for (int eb = 0; eb < 4; ++eb) of[eb] = lr16(aO + eb * 1024);
      MEMB;
#pragma unroll
      for (int eb = 0; eb < 4; ++eb) { SMV(d0, of[eb], Wo[eb][0]); SMV(d1, of[eb], Wo[eb][1]); }
      asm volatile("s_nop 6" : "+v"(d0));
      u32x2 o0 = pk4(d0); PIN(o0); PIN(d1);
      u32x2 o1 = pk4(d1);
      st16(u32x4{nosent(o0.x), nosent(o0.y), nosent(o1.x), nosent(o1.y)}, rw, vPst, O_RING + (s & 1) * 524288);
      MEMB;
      CYC(6);
      asm volatile("s_waitcnt vmcnt(1)\n\tds_write_b128 %4, %0\n\tds_write_b128 %4, %1 offset:1024\n\tds_write_b128 %5, %2\n\tds_write_b128 %5, %3 offset:1024"
                   :: "v"(kn0), "v"(kn1), "v"(vn0), "v"(vn1), "v"(aK + w * 2048), "v"(aV) : "memory");
    }
    if (r == 0) STAMP(16 + s);
    if (s < 3) STAMP(728 + s * 64 + r);
    if (s < 62 && prog < s + 3) {
      const u32 v = __builtin_amdgcn_readfirstlane(ld4(rw, O_CTRL + (F_KV + ((s + 2) >> 5) * 48 + h) * 128, 0));
      if (v && prog >= ((s + 2) & 32)) prog = v + ((s + 2) & 32);
    } else delay<D1>();
    CYC(7);
    f32x4 t;
    spins = 0;
#pragma unroll 1
    do {
      MEMB;
      const u32x4 pp = ld16(rw, vPld, O_RING + (s & 1) * 524288);
      np1 += s >= 20;
      MM0(t, sel, pp);
      ok = nosentinel(umax3(umax3(pp.x, pp.y, pp.z), pp.w, pp.w));
    } while (!ok && ++spins < SPIN_CAP);
    if (!ok) { st4(3, rw, O_CTRL + F_TMO * 128, 0); halt(); }
    CYC(8);
#define RA(i, n) "v_add_f32_dpp %" #i ", %" #i ", %" #i " row_ror:" #n " row_mask:0xf bank_mask:0xf\n\t"
    asm volatile("s_nop 3\n\t" RA(0, 2) RA(1, 2) RA(2, 2) RA(3, 2) RA(0, 4) RA(1, 4) RA(2, 4) RA(3, 4) RA(0, 8) RA(1, 8) RA(2, 8) RA(3, 8)
                 : "+v"(t[0]), "+v"(t[1]), "+v"(t[2]), "+v"(t[3]));
    u32x2 mo = pk4(t);
    if ((l & 0x2e) == 0) st8(u32x2{nosent(mo.x), nosent(mo.y)}, rw, vMst, O_MEM + (s + 1) * 16384);
    MEMB;
    CYC(9);
    st16(u32x4{~0u, ~0u, ~0u, ~0u}, rw, vPld, O_RING + (s & 1) * 524288);
  }
  waitvm<0>();
  bar();
  if (r == 0) { STAMP(3); STAMP(79); PROFV(8, np1); PROFV(9, np2); PROFV(10, side); }
  if (tid == 0) st4(64, rw, O_CTRL + (F_KM + rr) * 128, 0);
}

template <int ROWS, int RB> DEV void stage_w(const char* g, size_t rowstride, int l, u32 dst) {
  constexpr int LPR = RB / 16, RPI = 64 / LPR;
  const char* a = g + (size_t)(l / LPR) * rowstride + (l % LPR) * 16;
#pragma unroll
  for (int i = 0; i < ROWS / RPI; ++i) dma16(a + (size_t)i * RPI * rowstride, dst + i * 1024);
}
DEV bool wg_wait(rsrc_t rw, LAS char* sm, int w, u32 flag, u32 target, bool active) {
  LAS u32* okw = (LAS u32*)(sm + 155584);
  if (w == 0) *okw = wait_flags(rw, flag, target, active);
  bar();
  const u32 ok = *okw;
  bar();
  return ok != 0;
}

DEV void phase_ln1(const Params& p, rsrc_t rw, LAS char* sm, int wk) {
  const int tid = threadIdx.x, l = tid & 63, w = tid >> 6;
  const float* mod = (const float*)(align_ws(p.wsraw) + O_MOD);
  f32x4 sa0 = *(const f32x4*)(mod + l * 8), sa1 = *(const f32x4*)(mod + l * 8 + 4);
  f32x4 ca0 = *(const f32x4*)(mod + 512 + l * 8), ca1 = *(const f32x4*)(mod + 512 + l * 8 + 4);
  u32x4 xv[4];
#pragma unroll
  for (int i = 0; i < 4; ++i) xv[i] = *(const u32x4*)(p.x + (size_t)(wk * 16 + w * 4 + i) * 512 + l * 8);
#pragma unroll
  for (int i = 0; i < 4; ++i) {
    float v[8], s = 0.f, q = 0.f;
#pragma unroll
    for (int j = 0; j < 4; ++j) { v[2 * j] = lo(xv[i][j]); v[2 * j + 1] = hi(xv[i][j]); s += v[2 * j] + v[2 * j + 1]; }
    s = sw32(sw16(ror_add<8>(ror_add<4>(ror_add<2>(ror_add<1>(s)))), false), false) * (1.f / 512);
#pragma unroll
    for (int j = 0; j < 8; ++j) { v[j] -= s; q += v[j] * v[j]; }
    q = sw32(sw16(ror_add<8>(ror_add<4>(ror_add<2>(ror_add<1>(q)))), false), false) * (1.f / 512);
    const float rs = rsqrtf(q + 1e-5f);
    u32x4 o;
#pragma unroll
    for (int j = 0; j < 4; ++j) {
      const float c0 = j < 2 ? ca0[2 * j] : ca1[2 * j - 4], c1 = j < 2 ? ca0[2 * j + 1] : ca1[2 * j - 3];
      const float a0 = j < 2 ? sa0[2 * j] : sa1[2 * j - 4], a1 = j < 2 ? sa0[2 * j + 1] : sa1[2 * j - 3];
      o[j] = pk(v[2 * j] * rs * (1.f + c0) + a0, v[2 * j + 1] * rs * (1.f + c1) + a1);
    }
    lw16(sm + 131072 + l * 256 + (((w * 4 + i) ^ l) & 15) * 16, o);
  }
  bar();
#pragma unroll
  for (int i = 0; i < 4; ++i) {
    const int ch = i * 256 + tid;
    st16(lr16(sm + 131072 + (ch >> 4) * 256 + ((ch ^ (ch >> 4)) & 15) * 16), rw, wk * 16384 + ch * 16, O_H);
  }
  waitvm<0>();
  bar();
  if (tid == 0) add_flag(align_ws(p.wsraw), F_H + (wk >> 1));
}

template <bool ISV> DEV bool qkv_loop(rsrc_t rw, LAS char* sm, const u32x4 (&W)[16][4], int rg, u32 obase, u32 flag) {
  const int tid = threadIdx.x, l = tid & 63, w = tid >> 6;
  const f32x4 z4 = {0.f, 0.f, 0.f, 0.f};
  u32x4 pf[8];
#pragma unroll
  for (int j = 0; j < 8; ++j) pf[j] = ld16(rw, w * 8192 + j * 1024 + l * 16, O_H + rg * 32 * 32768);
#pragma unroll
  for (int j = 0; j < 8; ++j) lw16(sm + w * 8192 + j * 1024 + l * 16, pf[j]);
  bar();
  for (int i = 0; i < 32; ++i) {
    const int seg = rg * 32 + i;
    if (i < 31) {
#pragma unroll
      for (int j = 0; j < 8; ++j) pf[j] = ld16(rw, w * 8192 + j * 1024 + l * 16, O_H + (seg + 1) * 32768);
    }
    LAS char* a = sm + (i & 1) * 32768 + l * 16;
    f32x4 acc[2][4];
#pragma unroll
    for (int t = 0; t < 4; ++t) { acc[0][t] = z4; acc[1][t] = z4; }
#pragma unroll
    for (int ks = 0; ks < 16; ++ks) {
      const u32x4 h0 = lr16(a + ks * 1024), h1 = lr16(a + 16384 + ks * 1024);
#pragma unroll
      for (int t = 0; t < 4; ++t) {
        acc[0][t] = ISV ? mma(h0, W[ks][t], acc[0][t]) : mma(W[ks][t], h0, acc[0][t]);
        acc[1][t] = ISV ? mma(h1, W[ks][t], acc[1][t]) : mma(W[ks][t], h1, acc[1][t]);
      }
    }
#pragma unroll
    for (int t = 0; t < 4; ++t) {
      const u32x4 o = ISV ? cat(pk4(acc[0][t]), pk4(acc[1][t])) : cat(pk4(acc[t & 1][t & 2]), pk4(acc[t & 1][(t & 2) + 1]));
      st16(o, rw, t * 1024 + l * 16, obase + seg * 262144);
    }
    if (i < 3 || i == 31) waitvm<0>(); else waitvm<4>();
    if (i < 31) {
#pragma unroll
      for (int j = 0; j < 8; ++j) lw16(sm + ((i + 1) & 1) * 32768 + w * 8192 + j * 1024 + l * 16, pf[j]);
    }
    bar();
    if (tid == 0) st4(i < 3 || i == 31 ? i + 1 : i, rw, O_CTRL + flag * 128, 0);
  }
  return true;
}

#define RG1_AFTER 1
DEV bool phase_qkv(const Params& p, rsrc_t rw, LAS char* sm, int wk) {
  const int tid = threadIdx.x, l = tid & 63, w = __builtin_amdgcn_readfirstlane(tid >> 6);
  const int cb = wk % 48, rg = wk / 48, g = l >> 4, q4 = (l >> 2) & 3, p4 = l & 3;
  const bool isv = cb < 32 && w >= 2;
  const int head = cb < 32 ? cb : 2 * (cb - 32) + (w >> 1);
  const __bf16* M = cb >= 32 ? p.wq : (isv ? p.wv : p.wk);
  const char* gsrc = (const char*)(M + head * 128 + (w & 1) * 64);
  const u32 dst = (u32)(size_t)sm + w * 32768;
  if (rg) {
    if (wk < 176) phase_ln1(p, rw, sm, wk - 48);
    if (rg > 1) return true;
    if (!wg_wait(rw, sm, w, F_KV + cb, RG1_AFTER, l == 0)) return false;
  }
  stage_w<128, 128>(gsrc, 8192, l, dst); stage_w<128, 128>(gsrc + 128 * 8192, 8192, l, dst + 16384);
  u32x4 W[16][4];
  LAS char* a = sm + w * 32768 + (8 * g + q4) * 128 + (isv ? p4 * 8 : p4 * 16);
  const int tstep = isv ? 32 : 8;
#pragma unroll
  for (int c = 0; c < 4; ++c) {
    if (c < 3) waitvm<16>(); else waitvm<0>();
#pragma unroll
    for (int k = 0; k < 4; ++k)
#pragma unroll
      for (int t = 0; t < 4; ++t) {
        LAS char* q = a + (c & 1) * 16384 + k * 4096 + (t >> 1) * 64 + (t & 1) * tstep;
        W[c * 4 + k][t] = cat(trd(q), trd(q + 512));
      }
    waitlds();
    if (c < 2) stage_w<128, 128>(gsrc + (size_t)(c + 2) * 128 * 8192, 8192, l, dst + (c & 1) * 16384);
  }
  if (wk == 0) STAMP(4);
  if (!wg_wait(rw, sm, w, F_H + rg * 32 + l, 2, l < 32)) return false;
  if (wk == 0) STAMP(5);
  const u32 obase = (cb >= 32 ? O_Q : (isv ? O_V : O_K)) + head * 8192 + (w & 1) * 4096;
  if (isv) qkv_loop<true>(rw, sm, W, rg, obase, F_KV + rg * 48 + cb);
  else qkv_loop<false>(rw, sm, W, rg, obase, F_KV + rg * 48 + cb);
  return true;
}

DEV void role_attn(const Params& p, rsrc_t rw, LAS char* sm, int set, int hp) {
  const int tid = threadIdx.x, l = tid & 63, w = __builtin_amdgcn_readfirstlane(tid >> 6);
  const int g = l >> 4, n = l & 15, q4 = (l >> 2) & 3, p4 = l & 3;
  const f32x4 z4 = {0.f, 0.f, 0.f, 0.f};
  const char* gsrc = (const char*)(p.wo + (size_t)hp * 256 * 512 + w * 128);
  const u32 dst = (u32)(size_t)sm + w * 32768;
  u32x4 W[8][8];
  stage_w<64, 256>(gsrc, 1024, l, dst); stage_w<64, 256>(gsrc + 64 * 1024, 1024, l, dst + 16384);
  LAS char* a = sm + w * 32768 + (4 * g + q4) * 256 + p4 * 16;
#pragma unroll
  for (int c = 0; c < 4; ++c) {
    if (c < 3) waitvm<16>(); else waitvm<0>();
#pragma unroll
    for (int k = 0; k < 2; ++k)
#pragma unroll
      for (int t = 0; t < 8; ++t) {
        LAS char* q = a + (c & 1) * 16384 + k * 8192 + (t >> 1) * 64 + (t & 1) * 8;
        W[c * 2 + k][t] = cat(trd(q), trd(q + 4096));
      }
    waitlds();
    if (c < 2) stage_w<64, 256>(gsrc + (c + 2) * 64 * 1024, 1024, l, dst + (c & 1) * 16384);
  }
  const int hd = hp * 2 + (w >> 1), qt = w & 1;
  const float sc = 0.08838834764831845f * 1.4426950408889634f;
  bar();
  for (int s = set; s < 64; s += 3) {
    {
      const u32 rgb = F_KV + (s >> 5) * 48;
      if (!wait_flags(rw, l < 1 ? rgb + hd : rgb + 32 + hp, (s & 31) + 1, l < 2)) halt();
    }
    const u32 vo = hd * 8192 + l * 16, so = s * 262144;
    u32x4 kf[4][2], qf[4], km[4], vf[4][2]; u32x2 vm[4];
#pragma unroll
    for (int eb = 0; eb < 4; ++eb) {
      qf[eb] = ld16(rw, vo + eb * 2048 + qt * 1024, O_Q + so);
      kf[eb][0] = ld16(rw, vo + eb * 2048, O_K + so); kf[eb][1] = ld16(rw, vo + eb * 2048 + 1024, O_K + so);
    }
#pragma unroll
    for (int eb = 0; eb < 4; ++eb) { vf[eb][0] = ld16(rw, vo + eb * 2048, O_V + so); vf[eb][1] = ld16(rw, vo + eb * 2048 + 1024, O_V + so); }
    f32x4 s0 = z4, s1 = z4, sm_ = z4;
#pragma unroll
    for (int eb = 0; eb < 4; ++eb) { s0 = mma(kf[eb][0], qf[eb], s0); s1 = mma(kf[eb][1], qf[eb], s1); }
    MEMB;
    if (!wait_flags(rw, F_KM + hd * 2 + l, s + 1, l < 2)) halt();
    if (s == 63 && hp == 0) STAMP(930);
#pragma unroll
    for (int eb = 0; eb < 4; ++eb) km[eb] = ld16(rw, hd * 2048 + eb * 512 + g * 128 + (l & 7) * 16, O_KM + s * 65536);
#pragma unroll
    for (int eb = 0; eb < 4; ++eb) vm[eb] = ld8(rw, hd * 2048 + eb * 512 + l * 8, O_VM + s * 65536);
#pragma unroll
    for (int eb = 0; eb < 4; ++eb) sm_ = mma(km[eb], qf[eb], sm_);
    float mx = -3.0e38f;
#pragma unroll
    for (int i = 0; i < 4; ++i) {
      s0[i] = (4 * g + i <= 16 * qt + n) ? s0[i] * sc : -1.4426950408889634e9f;
      s1[i] = (16 + 4 * g + i <= 16 * qt + n) ? s1[i] * sc : -1.4426950408889634e9f;
      sm_[i] *= sc;
      mx = fmaxf(mx, fmaxf(fmaxf(s0[i], s1[i]), sm_[i]));
    }
    mx = sw32(sw16(mx, true), true);
    float sum = 0.f, summ = 0.f;
#pragma unroll
    for (int i = 0; i < 4; ++i) {
      s0[i] = __builtin_amdgcn_exp2f(s0[i] - mx); s1[i] = __builtin_amdgcn_exp2f(s1[i] - mx); sm_[i] = __builtin_amdgcn_exp2f(sm_[i] - mx);
      sum += s0[i] + s1[i]; summ += sm_[i];
    }
    const float inv = __builtin_amdgcn_rcpf(sw32(sw16(sum + 0.5f * summ, false), false));
    const u32x4 pf = cat(pk4(s0), pk4(s1));
    const u32x2 pm = pk4(sm_), zz = {0u, 0u}, pmx = g < 2 ? pm : zz, pmy = g < 2 ? zz : pm;
#pragma unroll
    for (int eb = 0; eb < 4; ++eb) {
      const f32x4 ox = mma16(vm[eb], pmx, mma(vf[eb][0], pf, z4)), oy = mma16(vm[eb], pmy, mma(vf[eb][1], pf, z4));
      lw16(sm + (qt * 8 + (w >> 1) * 4 + eb) * 1024 + l * 16, cat(pk4(ox * inv), pk4(oy * inv)));
    }
    bar();
#pragma unroll
    for (int t2 = 0; t2 < 2; ++t2) {
      f32x4 acc[8];
#pragma unroll
      for (int t = 0; t < 8; ++t) acc[t] = z4;
#pragma unroll
      for (int ks = 0; ks < 8; ++ks) {
        const u32x4 of = lr16(sm + (t2 * 8 + ks) * 1024 + l * 16);
#pragma unroll
        for (int t = 0; t < 8; ++t) acc[t] = mma(W[ks][t], of, acc[t]);
      }
#pragma unroll
      for (int u = 0; u < 4; ++u)
        st16(cat(pk4(acc[2 * u]), pk4(acc[2 * u + 1])), rw, hp * 32768 + t2 * 16384 + (w * 4 + u) * 1024 + l * 16, O_P1 + s * 524288);
    }
    waitvm<0>();
    bar();
    if (tid == 0) add_flag(align_ws(p.wsraw), F_P1 + s);
    if (s == 63 && hp == 0) STAMP(931);
  }
}

DEV void role_norm(const Params& p, rsrc_t rw, LAS char* sm, int set, int j) {
  const int tid = threadIdx.x, l = tid & 63, w = __builtin_amdgcn_readfirstlane(tid >> 6), t4 = tid & 3, pc = tid >> 2;
  const float* mod = (const float*)(align_ws(p.wsraw) + O_MOD) + pc * 8;
  float ga[8], sb[8], cb[8];
#pragma unroll
  for (int i = 0; i < 8; ++i) { ga[i] = mod[1024 + i]; sb[i] = mod[1536 + i]; cb[i] = 1.f + mod[2048 + i]; }
  LAS float* red = (LAS float*)sm;
  const u32 vo = (j >> 2) * 16384 + pc * 256 + ((4 * j + t4) & 15) * 16;
  for (int s = set; s < 64; s += 3) {
    const u32x4 xv = *(const u32x4*)(p.x + (size_t)(s * 32 + 4 * j + t4) * 512 + pc * 8);
    if (!wait_flags(rw, F_P1 + s, 16, l < 1)) halt();
    if (s == 63 && j == 0) STAMP(932);
    u32x4 pp[16];
#pragma unroll
    for (int i = 0; i < 16; ++i) pp[i] = ld16(rw, vo + i * 32768, O_P1 + s * 524288);
    float v[8] = {0, 0, 0, 0, 0, 0, 0, 0};
#pragma unroll
    for (int i = 0; i < 16; ++i)
#pragma unroll
      for (int e = 0; e < 4; ++e) { v[2 * e] += lo(pp[i][e]); v[2 * e + 1] += hi(pp[i][e]); }
    u32x4 x1; float su = 0.f, q = 0.f;
#pragma unroll
    for (int e = 0; e < 4; ++e) {
      x1[e] = pk(lo(xv[e]) + ga[2 * e] * rb(v[2 * e]), hi(xv[e]) + ga[2 * e + 1] * rb(v[2 * e + 1]));
      v[2 * e] = lo(x1[e]); v[2 * e + 1] = hi(x1[e]); su += v[2 * e] + v[2 * e + 1];
    }
    st16(x1, rw, vo, O_X1 + s * 32768);
    su = sw32(sw16(ror_add<8>(ror_add<4>(su)), false), false);
    if (l < 4) red[w * 4 + l] = su;
    bar();
    su = (red[t4] + red[4 + t4] + red[8 + t4] + red[12 + t4]) * (1.f / 512);
#pragma unroll
    for (int e = 0; e < 8; ++e) { v[e] -= su; q += v[e] * v[e]; }
    q = sw32(sw16(ror_add<8>(ror_add<4>(q)), false), false);
    if (l < 4) red[16 + w * 4 + l] = q;
    bar();
    const float rs = rsqrtf((red[16 + t4] + red[20 + t4] + red[24 + t4] + red[28 + t4]) * (1.f / 512) + 1e-5f);
    u32x4 o;
#pragma unroll
    for (int e = 0; e < 4; ++e) o[e] = pk(v[2 * e] * rs * cb[2 * e] + sb[2 * e], v[2 * e + 1] * rs * cb[2 * e + 1] + sb[2 * e + 1]);
    st16(o, rw, vo, O_H2 + s * 32768);
    waitvm<0>();
    bar();
    if (tid == 0) add_flag(align_ws(p.wsraw), F_H2 + s);
    if (s == 63 && j == 0) STAMP(933);
  }
}

DEV float gelu(float x) {
  const float u = x + 0.044715f * x * x * x;
  return x * __builtin_amdgcn_rcpf(1.f + __builtin_amdgcn_exp2f(u * (-2.f * 0.7978845608028654f * 1.4426950408889634f)));
}
DEV void role_mlp(const Params& p, rsrc_t rw, LAS char* sm, int set, int fs) {
  const int tid = threadIdx.x, l = tid & 63, w = __builtin_amdgcn_readfirstlane(tid >> 6);
  const int g = l >> 4, q4 = (l >> 2) & 3, p4 = l & 3;
  const f32x4 z4 = {0.f, 0.f, 0.f, 0.f};
  const bool pad = fs == 10 && w == 3;
  const int f0 = pad ? 0 : fs * 128 + w * 32;
  const u32 dst = (u32)(size_t)sm + w * 32768;
  u32x4 W1[16][2], W2[4][8];
  const char* g1 = (const char*)(p.w1 + f0);
  const char* g2 = (const char*)(p.w2 + (size_t)fs * 128 * 512 + w * 128);
  stage_w<256, 64>(g1, 2752, l, dst); stage_w<256, 64>(g1 + 256 * 2752, 2752, l, dst + 16384);
#pragma unroll
  for (int c = 0; c < 2; ++c) {
    waitvm<16>();
    LAS char* a = sm + w * 32768 + c * 16384 + (8 * g + q4) * 64 + p4 * 16;
#pragma unroll
    for (int k = 0; k < 8; ++k)
#pragma unroll
      for (int t = 0; t < 2; ++t) W1[c * 8 + k][t] = cat(trd(a + k * 2048 + t * 8), trd(a + k * 2048 + t * 8 + 256));
    waitlds();
    stage_w<32, 256>(g2 + c * 64 * 1024, 1024, l, dst + c * 16384);
    stage_w<32, 256>(g2 + ((c == 1 && fs == 10) ? 0 : (c * 64 + 32) * 1024), 1024, l, dst + c * 16384 + 8192);
  }
#pragma unroll
  for (int c = 0; c < 2; ++c) {
    if (c == 0) waitvm<16>(); else waitvm<0>();
    LAS char* a = sm + w * 32768 + c * 16384 + (8 * g + q4) * 256 + p4 * 16;
#pragma unroll
    for (int k = 0; k < 2; ++k)
#pragma unroll
      for (int t = 0; t < 8; ++t) {
        LAS char* q = a + k * 8192 + (t >> 1) * 64 + (t & 1) * 8;
        W2[c * 2 + k][t] = cat(trd(q), trd(q + 1024));
      }
  }
  if (fs == 10) {
#pragma unroll
    for (int t = 0; t < 8; ++t) W2[3][t] = u32x4{0u, 0u, 0u, 0u};
  }
  const u32x4 b1v = *(const u32x4*)(p.b1 + f0 + g * 8);
  bar();
  for (int s = set; s < 64; s += 3) {
    if (!wait_flags(rw, F_H2 + s, 8, l < 1)) halt();
    if (s == 63 && fs == 0) STAMP(934);
    u32x4 pf[8];
#pragma unroll
    for (int j = 0; j < 8; ++j) pf[j] = ld16(rw, w * 8192 + j * 1024 + l * 16, O_H2 + s * 32768);
#pragma unroll
    for (int j = 0; j < 8; ++j) lw16(sm + w * 8192 + j * 1024 + l * 16, pf[j]);
    bar();
#pragma unroll
    for (int t2 = 0; t2 < 2; ++t2) {
      f32x4 a0 = {lo(b1v[0]), hi(b1v[0]), lo(b1v[1]), hi(b1v[1])}, a1 = {lo(b1v[2]), hi(b1v[2]), lo(b1v[3]), hi(b1v[3])};
#pragma unroll
      for (int ks = 0; ks < 16; ++ks) {
        const u32x4 hf = lr16(sm + t2 * 16384 + ks * 1024 + l * 16);
        a0 = mma(W1[ks][0], hf, a0); a1 = mma(W1[ks][1], hf, a1);
      }
#pragma unroll
      for (int i = 0; i < 4; ++i) { a0[i] = pad ? 0.f : gelu(rb(a0[i])); a1[i] = pad ? 0.f : gelu(rb(a1[i])); }
      lw16(sm + 32768 + (t2 * 4 + w) * 1024 + l * 16, cat(pk4(a0), pk4(a1)));
    }
    bar();
#pragma unroll
    for (int t2 = 0; t2 < 2; ++t2) {
      f32x4 acc[8];
#pragma unroll
      for (int t = 0; t < 8; ++t) acc[t] = z4;
#pragma unroll
      for (int ks = 0; ks < 4; ++ks) {
        const u32x4 gf = lr16(sm + 32768 + (t2 * 4 + ks) * 1024 + l * 16);
#pragma unroll
        for (int t = 0; t < 8; ++t) acc[t] = mma(W2[ks][t], gf, acc[t]);
      }
#pragma unroll
      for (int u = 0; u < 4; ++u)
        st16(cat(pk4(acc[2 * u]), pk4(acc[2 * u + 1])), rw, fs * 32768 + t2 * 16384 + (w * 4 + u) * 1024 + l * 16, O_P2 + s * 360448);
    }
    waitvm<0>();
    bar();
    if (tid == 0) add_flag(align_ws(p.wsraw), F_P2 + s);
    if (s == 63 && fs == 0) STAMP(935);
  }
}

DEV void role_out(const Params& p, rsrc_t rw, LAS char* sm, int set, int j) {
  const int tid = threadIdx.x, l = tid & 63, w = __builtin_amdgcn_readfirstlane(tid >> 6), t4 = tid & 3, pc = tid >> 2;
  const float* mod = (const float*)(align_ws(p.wsraw) + O_MOD) + pc * 8;
  float gb[8];
#pragma unroll
  for (int i = 0; i < 8; ++i) gb[i] = mod[2560 + i];
  const u32x4 b2v = *(const u32x4*)(p.b2 + pc * 8);
  const u32 vo = (j >> 2) * 16384 + pc * 256 + ((4 * j + t4) & 15) * 16;
  for (int s = set; s < 64; s += 3) {
    if (!wait_flags(rw, F_P2 + s, 11, l < 1)) halt();
    if (s == 63 && j == 0) STAMP(936);
    u32x4 pp[11];
#pragma unroll
    for (int i = 0; i < 11; ++i) pp[i] = ld16(rw, vo + i * 32768, O_P2 + s * 360448);
    const u32x4 x1 = ld16(rw, vo, O_X1 + s * 32768);
    u32x4 o;
#pragma unroll
    for (int e = 0; e < 4; ++e) {
      float a = 0.f, b = 0.f;
#pragma unroll
      for (int i = 0; i < 11; ++i) { a += lo(pp[i][e]); b += hi(pp[i][e]); }
      o[e] = pk(lo(x1[e]) + gb[2 * e] * rb(rb(a) + lo(b2v[e])), hi(x1[e]) + gb[2 * e + 1] * rb(rb(b) + hi(b2v[e])));
    }
    *(u32x4*)(p.out + (size_t)(s * 32 + 4 * j + t4) * 512 + pc * 8) = o;
  }
}

__global__ __launch_bounds__(256, 1) void main_kernel(Params p) {
  __shared__ __attribute__((aligned(16))) char smem[L_END];
  LAS char* sm = (LAS char*)smem;
  const int b = blockIdx.x, i4 = (b >> 3) * 4 + (b & 3);
  if ((b & 4) == 0 && b < 128) { role_recur(p, sm, i4); return; }
  const int wk = (b & 4) ? i4 : 64 + i4;
  rsrc_t rw = mkrsrc(align_ws(p.wsraw), WS_END);
  if (!phase_qkv(p, rw, sm, wk)) return;
  STAMP(128 + wk);
  if (wk < 48) role_attn(p, rw, sm, wk / 16, wk % 16);
  else if (wk < 81) role_mlp(p, rw, sm, (wk - 48) / 11, (wk - 48) % 11);
  else if (wk < 105) role_norm(p, rw, sm, (wk - 81) / 8, (wk - 81) % 8);
  else if (wk >= 128 && wk < 152) role_out(p, rw, sm, (wk - 128) / 8, (wk - 128) % 8);
  STAMP(384 + wk);
}

extern "C" void kernel_launch(void* const* d_in, const int* in_sizes, int n_in, void* d_out, int out_size, void* d_ws,
                              size_t ws_size, hipStream_t stream) {
  Params p;
  p.x = (const __bf16*)d_in[0]; p.mem0 = (const __bf16*)d_in[1]; p.wq = (const __bf16*)d_in[2]; p.wk = (const __bf16*)d_in[3];
  p.wv = (const __bf16*)d_in[4]; p.wo = (const __bf16*)d_in[5]; p.w1 = (const __bf16*)d_in[8]; p.b1 = (const __bf16*)d_in[9];
  p.w2 = (const __bf16*)d_in[10]; p.b2 = (const __bf16*)d_in[11]; p.c = (const __bf16*)d_in[12]; p.wmod = (const __bf16*)d_in[13];
  p.out = (__bf16*)d_out;
  p.wsraw = (char*)d_ws;
  prologue_kernel<<<256, 256, 0, stream>>>(p);
  main_kernel<<<256, 256, 0, stream>>>(p);
}
```

```cpp
#include <hip/hip_runtime.h>
#include <hip/hip_bf16.h>

typedef unsigned u32;
typedef unsigned u32x2 __attribute__((ext_vector_type(2)));
typedef unsigned u32x4 __attribute__((ext_vector_type(4)));
typedef unsigned u32x8 __attribute__((ext_vector_type(8)));
typedef float f32x2 __attribute__((ext_vector_type(2)));
typedef float f32x4 __attribute__((ext_vector_type(4)));
typedef short s16x4 __attribute__((ext_vector_type(4)));
typedef __bf16 bf2 __attribute__((ext_vector_type(2)));
typedef __bf16 bf8 __attribute__((ext_vector_type(8)));
typedef __bf16 bf16v __attribute__((ext_vector_type(16)));
typedef __amdgpu_buffer_rsrc_t rsrc_t;
#define LAS __attribute__((address_space(3)))
#define DEV __device__ __forceinline__
constexpr int SC1 = 16;
constexpr u32 SPIN_CAP = 400000;

constexpr u32 MB = 1u << 20;
constexpr u32 O_MEM = 0;
constexpr u32 O_RING = 1 * MB;
constexpr u32 O_CTRL = 2 * MB;
constexpr u32 O_MOD = 2 * MB + 131072;
constexpr u32 O_H = 4 * MB, O_X1 = 6 * MB, O_H2 = 8 * MB, O_KM = 10 * MB, O_VM = 14 * MB;
constexpr u32 O_Q = 18 * MB, O_K = 34 * MB, O_V = 50 * MB, O_P1 = 66 * MB, O_P2 = 98 * MB, WS_END = 120 * MB;
constexpr u32 F_TMO = 0, F_SIDE = 1, F_H = 2, F_KV = F_H + 64, F_KM = F_KV + 192, F_P1 = F_KM + 64, F_H2 = F_P1 + 64,
              F_P2 = F_H2 + 64, F_END = F_P2 + 64;

struct Params {
  const __bf16 *x, *mem0, *wq, *wk, *wv, *wo, *w1, *b1, *w2, *b2, *c, *wmod;
  __bf16* out;
  char* wsraw;
};

DEV char* align_ws(char* p) { return p + ((0 - (size_t)p) & (2 * MB - 1)); }
DEV rsrc_t mkrsrc(const void* p, u32 n) { return __builtin_amdgcn_make_buffer_rsrc((void*)p, 0, n, 0x00020000); }
DEV u32x4 ld16(rsrc_t r, u32 v, u32 s) { return __builtin_amdgcn_raw_buffer_load_b128(r, v, s, SC1); }
DEV u32x2 ld8(rsrc_t r, u32 v, u32 s) { return __builtin_amdgcn_raw_buffer_load_b64(r, v, s, SC1); }
DEV u32 ld4(rsrc_t r, u32 v, u32 s) { return __builtin_amdgcn_raw_buffer_load_b32(r, v, s, SC1); }
DEV void st16(u32x4 d, rsrc_t r, u32 v, u32 s) { __builtin_amdgcn_raw_buffer_store_b128(d, r, v, s, SC1); }
DEV void st8(u32x2 d, rsrc_t r, u32 v, u32 s) { __builtin_amdgcn_raw_buffer_store_b64(d, r, v, s, SC1); }
DEV void st4(u32 d, rsrc_t r, u32 v, u32 s) { __builtin_amdgcn_raw_buffer_store_b32(d, r, v, s, SC1); }
DEV u32 pk(float a, float b) { f32x2 v = {a, b}; return __builtin_bit_cast(u32, __builtin_convertvector(v, bf2)); }
DEV float lo(u32 v) { return __uint_as_float(v << 16); }
DEV float hi(u32 v) { return __uint_as_float(v & 0xffff0000u); }
DEV float rb(float a) { return lo(pk(a, a)); }
DEV u32x2 trd(LAS char* p) { return __builtin_bit_cast(u32x2, __builtin_amdgcn_ds_read_tr16_b64_v4i16((LAS s16x4*)p)); }
DEV u32x4 lr16(LAS char* p) { return *(LAS u32x4*)p; }
DEV void lw16(LAS char* p, u32x4 v) { *(LAS u32x4*)p = v; }
DEV void lw8(LAS char* p, u32x2 v) { *(LAS u32x2*)p = v; }
DEV u32x4 cat(u32x2 a, u32x2 b) { return u32x4{a.x, a.y, b.x, b.y}; }
DEV u32x8 cat(u32x4 a, u32x4 b) { return u32x8{a.x, a.y, a.z, a.w, b.x, b.y, b.z, b.w}; }
DEV f32x4 mma(u32x4 a, u32x4 b, f32x4 c) {
  return __builtin_amdgcn_mfma_f32_16x16x32_bf16(__builtin_bit_cast(bf8, a), __builtin_bit_cast(bf8, b), c, 0, 0, 0);
}
DEV f32x4 mma16(u32x2 a, u32x2 b, f32x4 c) {
  return __builtin_amdgcn_mfma_f32_16x16x16bf16_1k(__builtin_bit_cast(s16x4, a), __builtin_bit_cast(s16x4, b), c, 0, 0, 0);
}
DEV f32x4 smm(u32x4 a, u32x8 b, f32x4 c, u32 idx) {
  return __builtin_amdgcn_smfmac_f32_16x16x64_bf16(__builtin_bit_cast(bf8, a), __builtin_bit_cast(bf16v, b), c, idx, 0, 0);
}
#define MEMB asm volatile("" ::: "memory")
DEV float max3(float a, float b, float c) { float r; asm("v_max3_f32 %0, %1, %2, %3" : "=v"(r) : "v"(a), "v"(b), "v"(c)); return r; }
DEV void bar() { asm volatile("s_waitcnt lgkmcnt(0)\n\ts_barrier" ::: "memory"); }
template <int N> DEV void waitvm() { asm volatile("s_waitcnt vmcnt(%0)" ::"i"(N) : "memory"); }
DEV void waitlds() { asm volatile("s_waitcnt lgkmcnt(0)" ::: "memory"); }
DEV void dma16(const void* g, u32 ldsaddr) {
  u32 keep;
  asm volatile("s_mov_b32 %0, m0\n\ts_mov_b32 m0, %2\n\ts_nop 0\n\tglobal_load_lds_dwordx4 %1, off\n\ts_mov_b32 m0, %0"
               : "=&s"(keep) : "v"(g), "s"(ldsaddr) : "memory");
}
DEV float sw16(float a, bool mx) {
  auto r = __builtin_amdgcn_permlane16_swap(__float_as_uint(a), __float_as_uint(a), false, false);
  float x = __uint_as_float(r[0]), y = __uint_as_float(r[1]);
  return mx ? fmaxf(x, y) : x + y;
}
DEV float sw32(float a, bool mx) {
  auto r = __builtin_amdgcn_permlane32_swap(__float_as_uint(a), __float_as_uint(a), false, false);
  float x = __uint_as_float(r[0]), y = __uint_as_float(r[1]);
  return mx ? fmaxf(x, y) : x + y;
}
template <int C> DEV float ror_add(float u) {
  return u + __int_as_float(__builtin_amdgcn_update_dpp(0, __float_as_int(u), 0x120 + C, 0xf, 0xf, true));
}
DEV f32x4 fold(f32x4 x, f32x4 y) {
  f32x4 o;
#pragma unroll
  for (int i = 0; i < 4; ++i) {
    auto r = __builtin_amdgcn_permlane32_swap(__float_as_uint(x[i]), __float_as_uint(y[i]), false, false);
    o[i] = __uint_as_float(r[0]) + __uint_as_float(r[1]);
  }
  return o;
}
DEV f32x4 vzero() { f32x4 z; asm volatile("v_mov_b32 %0, 0" : "=v"(z[0])); asm volatile("v_mov_b32 %0, 0" : "=v"(z[1])); asm volatile("v_mov_b32 %0, 0" : "=v"(z[2])); asm volatile("v_mov_b32 %0, 0" : "=v"(z[3])); return z; }
DEV u32x2 pk4(f32x4 v) { return u32x2{pk(v[0], v[1]), pk(v[2], v[3])}; }
DEV u32 nosent(u32 v) { return v < 0xfffffffeu ? v : 0xfffffffeu; }
DEV bool valid(u32x4 a) { return a.x != ~0u && a.y != ~0u && a.z != ~0u && a.w != ~0u; }
DEV u32 nearoff(u32 off, u32 side) { return ((off >> 13) << 14) | (off & 8191) | (side << 13); }
DEV u32 memoff(u32 k, u32 quad) {
  u32 hb = (k >> 4) & 1;
  return (k >> 6) * 1024 + hb * 512 + quad * 256 + ((k & 15) ^ (8 * quad)) * 16 + (((k >> 5) & 1) ^ hb) * 8;
}
template <int N> DEV void delay() {
#pragma unroll
  for (int i = 0; i < N; ++i) asm volatile("s_nop 7" ::: "memory");
}
DEV u32 umax3(u32 a, u32 b, u32 c) { u32 r; asm("v_max3_u32 %0, %1, %2, %3" : "=v"(r) : "v"(a), "v"(b), "v"(c)); return r; }
DEV float fadd(float a, float b) { float r; asm("v_add_f32 %0, %1, %2" : "=v"(r) : "v"(a), "v"(b)); return r; }
DEV float fmul(float a, float b) { float r; asm("v_mul_f32 %0, %1, %2" : "=v"(r) : "v"(a), "v"(b)); return r; }
DEV f32x4 add4(f32x4 a, f32x4 b) { return f32x4{fadd(a[0], b[0]), fadd(a[1], b[1]), fadd(a[2], b[2]), fadd(a[3], b[3])}; }
DEV f32x4 mul4(f32x4 a, float b) { return f32x4{fmul(a[0], b), fmul(a[1], b), fmul(a[2], b), fmul(a[3], b)}; }
DEV void halt() { asm volatile("s_endpgm" ::: "memory"); __builtin_unreachable(); }
DEV bool nosentinel(u32 m) { return __builtin_amdgcn_ballot_w64(m == ~0u) == 0; }
#define SLP 8
DEV u32 wait_flags(rsrc_t rw, u32 flag, u32 target, bool active, bool fast = false) {
  for (u32 spins = 0;; ++spins) {
    asm volatile("" ::: "memory");
    u32 v = ld4(rw, O_CTRL + (active ? flag : F_TMO) * 128, 0);
    if (__all(!active || v >= target)) return __builtin_amdgcn_readfirstlane(v);
    if (spins > SPIN_CAP || __any(!active && v != 0)) { st4(1, rw, O_CTRL + F_TMO * 128, 0); return 0; }
    if (fast) __builtin_amdgcn_s_sleep(1); else __builtin_amdgcn_s_sleep(SLP);
  }
}
#define STAMP(i)
#define PROFV(i, v)
#define CYC(i)
DEV void add_flag(char* ws, u32 flag) {
  __hip_atomic_fetch_add((__attribute__((address_space(1))) u32*)(ws + O_CTRL + flag * 128), 1u, __ATOMIC_RELAXED, __HIP_MEMORY_SCOPE_AGENT);
}

#define NPROBE 3
__global__ __launch_bounds__(256) void prologue_kernel(Params p) {
  __shared__ float red[4][32];
  const int tid = threadIdx.x, b = blockIdx.x, l = tid & 63, w = tid >> 6;
  if (b == 0) STAMP(0);
  rsrc_t rw = mkrsrc(align_ws(p.wsraw), WS_END);
  for (u32 j = b * 256 + tid; j < 2 * MB / 16; j += 65536) {
    u32x4 v = {~0u, ~0u, ~0u, ~0u};
    if (j < 1024) {
      u32 ch = j & 511, hb = (ch >> 5) & 1, quad = (ch >> 4) & 1, kk = (ch >> 6) * 64 + hb * 16 + ((ch & 15) ^ (8 * quad));
      const unsigned short* m = (const unsigned short*)p.mem0 + quad * 4 * 512;
      u32 ka = kk + hb * 32, kb = kk + (hb ^ 1) * 32;
      v = u32x4{nosent(m[ka] | (u32)m[512 + ka] << 16), nosent(m[1024 + ka] | (u32)m[1536 + ka] << 16),
                nosent(m[kb] | (u32)m[512 + kb] << 16), nosent(m[1024 + kb] | (u32)m[1536 + kb] << 16)};
    }
    st16(v, rw, j * 16, 0);
  }
  if (b >= 96 && b < 99) { u32 f = (b - 96) * 256 + tid; if (f < F_END) st4(0, rw, O_CTRL + f * 128, 0); }
  if (b < 96) {
    const int kq = tid >> 2, cq = tid & 3;
    u32x4 cv = *(const u32x4*)(p.c + kq * 8);
    float acc[8] = {0, 0, 0, 0, 0, 0, 0, 0};
#pragma unroll
    for (int i = 0; i < 8; ++i) {
      float cc = (i & 1) ? hi(cv[i >> 1]) : lo(cv[i >> 1]);
      float s = rb(cc / (1.f + __expf(-cc)));
      u32x4 wv = *(const u32x4*)(p.wmod + (size_t)(kq * 8 + i) * 3072 + b * 32 + cq * 8);
#pragma unroll
      for (int j = 0; j < 4; ++j) { acc[2 * j] += s * lo(wv[j]); acc[2 * j + 1] += s * hi(wv[j]); }
    }
#pragma unroll
    for (int j = 0; j < 8; ++j) { float t = sw32(sw16(ror_add<8>(ror_add<4>(acc[j])), false), false); if (l < 4) red[w][l * 8 + j] = t; }
    __syncthreads();
    if (tid < 32) ((float*)(align_ws(p.wsraw) + O_MOD))[b * 32 + tid] = rb(red[0][tid] + red[1][tid] + red[2][tid] + red[3][tid]);
  }
}

constexpr u32 L_MEM = 131072, L_K = L_MEM + 8192, L_Q = L_K + 8192, L_KM = L_Q + 2048, L_O = L_KM + 2048, L_V = L_O + 4096, L_END = L_V + 8192;
#define D1 5
#define D2 18

DEV void stage_qkv(const Params& p, int i, int h, int w, int l, u32 smb) {
  const __bf16* W = i < 4 ? p.wq : (i < 8 ? p.wv : p.wk);
#pragma unroll
  for (int j = 0; j < 8; ++j) {
    const int row = w * 32 + j * 4 + (l >> 4), sw = 2 * ((l >> 4) | ((j >> 1) & 1) << 2);
    dma16((const char*)(W + (size_t)((i & 3) * 128 + row) * 4096 + h * 128) + ((l & 15) ^ sw) * 16, smb + (i & 3) * 32768 + w * 8192 + j * 1024);
  }
}
DEV void read_qkv(LAS char* src, int w, int l, u32x8 (&W)[8][2], int B0) {
  const int g = l >> 4, q4 = (l >> 2) & 3, p4 = l & 3, sw = 2 * (q4 | (g & 1) << 2);
#pragma unroll
  for (int t = 0; t < 2; ++t) {
    LAS char* a = src + (g * 8 + q4) * 256 + ((w * 4 + t * 2 + (p4 >> 1)) ^ sw) * 16 + (p4 & 1) * 8;
#pragma unroll
    for (int B = 0; B < 2; ++B) {
      LAS char* q = a + B * 16384;
      W[B0 + B][t] = cat(cat(trd(q), trd(q + 1024)), cat(trd(q + 8192), trd(q + 9216)));
    }
  }
}

DEV void role_recur(const Params& p, LAS char* sm, int r) {
  const int tid = threadIdx.x, l = tid & 63, w = __builtin_amdgcn_readfirstlane(tid >> 6);
  const int h = (r & 3) + 4 * (r >> 3), c = (r >> 2) & 1, rr = h * 2 + c;
  const int g = l >> 4, n = l & 15, q4 = (l >> 2) & 3, p4 = l & 3;
  rsrc_t rw = mkrsrc(align_ws(p.wsraw), WS_END);
  if (r == 0) {
    const char* q = align_ws(p.wsraw) + O_MEM + 4096 + w * 1024;
    u32 best[2] = {~0u, ~0u}, d;
    asm volatile("global_load_dword %0, %1, off sc1\n\ts_waitcnt vmcnt(0)" : "=v"(d) : "v"(q - 4096) : "memory");
#pragma unroll 1
    for (int i = 0; i < NPROBE; ++i)
#pragma unroll
      for (int e = 0; e < 2; ++e) {
        unsigned long long t1, t2;
        asm volatile("s_memtime %0\n\ts_waitcnt lgkmcnt(0)\n\tglobal_load_dword %2, %3, off sc1\n\ts_waitcnt vmcnt(0)\n\ts_memtime %1\n\ts_waitcnt lgkmcnt(0)"
                     : "=&s"(t1), "=&s"(t2), "=&v"(d) : "v"(q + e * 8192 + i * 256) : "memory");
        best[e] = min(best[e], (u32)(t2 - t1));
      }
    LAS u32* lat = (LAS u32*)(sm + L_MEM);
    lat[w * 2] = best[0]; lat[w * 2 + 1] = best[1];
    bar();
    const u32 l0 = min(min(lat[0], lat[2]), min(lat[4], lat[6])), l1 = min(min(lat[1], lat[3]), min(lat[5], lat[7]));
    if (tid == 0) st4(l0 <= l1 ? 1u : 2u, rw, O_CTRL + F_SIDE * 128, 0);
    PROFV(11, l0 * 10000 + l1);
    STAMP(12);
  }
  const u32 smb = (u32)(size_t)sm;
  LAS char* stA = sm + w * 32768; LAS char* stB = stA + 16384;
  if (r == 0) STAMP(1);
  u32 np1 = 0, np2 = 0;
  u32x8 Wq[8][2], Wv[8][2], Wk[8][2];
  stage_qkv(p, 0, h, w, l, smb); stage_qkv(p, 1, h, w, l, smb); stage_qkv(p, 2, h, w, l, smb);
#pragma unroll
  for (int i = 0; i < 12; ++i) {
    if (i == 10) waitvm<8>(); else waitvm<16>();
    bar();
    if (i < 9) stage_qkv(p, i + 3, h, w, l, smb);
    if (i == 10) {
      const char* gp = (const char*)(p.wo + (size_t)(h * 128 + (l >> 3)) * 512 + c * 256 + w * 64) + (l & 7) * 16;
#pragma unroll
      for (int j = 0; j < 16; ++j) dma16(gp + j * 8 * 1024, smb + w * 16384 + j * 1024);
    }
    read_qkv(sm + (i & 3) * 32768, w, l, i < 4 ? Wq : (i < 8 ? Wv : Wk), (i & 3) * 2);
    waitlds();
  }
  {
    u32x8 T[4][2];
    waitvm<0>();
    LAS char* a = sm + w * 16384 + (16 * (g & 1) + 4 * (g >> 1) + (q4 & 1)) * 128 + (q4 >> 1) * 32 + p4 * 8;
#pragma unroll
    for (int eb = 0; eb < 4; ++eb)
#pragma unroll
      for (int t = 0; t < 2; ++t) {
        LAS char* q = a + eb * 4096 + t * 64;
        T[eb][t] = cat(cat(trd(q), trd(q + 256)), cat(trd(q + 1024), trd(q + 1280)));
      }
    bar();
#pragma unroll
    for (int B = 0; B < 4; ++B)
#pragma unroll
      for (int t = 0; t < 2; ++t) {
        const u32x8 x = T[B][t], y = Wk[4 + B][t];
        lw16(stA + (B * 4 + t * 2) * 1024 + l * 16, u32x4{x[0], x[1], x[2], x[3]});
        lw16(stA + (B * 4 + t * 2 + 1) * 1024 + l * 16, u32x4{x[4], x[5], x[6], x[7]});
        lw16(stB + (B * 4 + t * 2) * 1024 + l * 16, u32x4{y[0], y[1], y[2], y[3]});
        lw16(stB + (B * 4 + t * 2 + 1) * 1024 + l * 16, u32x4{y[4], y[5], y[6], y[7]});
      }
  }
  u32 side = wait_flags(rw, F_SIDE, 1, l == 0);
  if (!side) halt();
  side = (side - 1) & 1;
  const u32 idx = (l & 8) ? 0xEEEEu : 0x4444u;
  u32x4 sel;
#pragma unroll
  for (int j = 0; j < 4; ++j) sel[j] = ((l & 7) == 2 * j ? 0x3f80u : 0u) | ((l & 7) == 2 * j + 1 ? 0x3f800000u : 0u);
  LAS char* aA0 = sm + L_MEM + memoff(16 * g + 4 * (q4 >> 1) + (q4 & 1) + 2 * (p4 >> 1), p4 & 1);
  LAS char* aA1 = sm + L_MEM + memoff(16 * g + 8 + 4 * (q4 >> 1) + (q4 & 1) + 2 * (p4 >> 1), p4 & 1);
  LAS char* aXw = sm + L_Q + w * 512 + (g & 1) * 256 + ((16 * (g >> 1) + n) ^ (4 * (g & 1))) * 8;
  LAS char* aX0 = sm + L_Q + (p4 & 1) * 256 + ((8 * g + q4) ^ (4 * (p4 & 1))) * 8;
  LAS char* aX1 = sm + L_Q + (p4 & 1) * 256 + ((8 * g + 4 + q4) ^ (4 * (p4 & 1))) * 8;
  const u32 vMem = side * 8192 + w * 2048 + l * 16;
  const u32 vPst = nearoff((c * 32 + w * 8 + (l >> 3)) * 4096 + h * 128 + (l & 7) * 16, side);
  const u32 vPld = nearoff(rr * 4096 + (l >> 1) * 128 + w * 32 + (l & 1) * 16, side);
  u32 vMst;
  {
    u32 ch = rr * 8 + w * 2 + (l & 1), gg = (ch >> 4) & 3;
    vMst = side * 8192 + memoff((ch >> 6) * 64 + 32 * (g & 1) + 16 * (gg >> 1) + (ch & 15), gg & 1);
  }
  const u32 vKV = h * 8192 + w * 2048 + l * 16, vKV2 = vKV + 1024;
  if (r == 0) STAMP(2);
  STAMP(600 + r);
  u32 prog = wait_flags(rw, F_KV + h, 1, l == 0);
  if (!prog) halt();
  STAMP(664 + r);
  u32x4 kn0 = ld16(rw, vKV, O_K), kn1 = ld16(rw, vKV + 1024, O_K), vn0 = ld16(rw, vKV, O_V), vn1 = ld16(rw, vKV + 1024, O_V);
  const f32x4 z4 = {0.f, 0.f, 0.f, 0.f};
  const u32x4 ones = {0x3f803f80u, 0x3f803f80u, 0x3f803f80u, 0x3f803f80u};
  const u32x2 ones2 = {0x3f803f80u, 0x3f803f80u};
  const float sc = 0.08838834764831845f * 1.4426950408889634f;

  LAS char* aWo = stA + l * 16; LAS char* aWk = stB + l * 16;
  LAS char* aK = sm + L_K + l * 16; LAS char* aO = sm + L_O + l * 16; LAS char* aM = sm + L_MEM + w * 2048 + l * 16;
  LAS char* aV = sm + L_V + w * 2048 + l * 16;
#define PIN(x) asm volatile("" : "+v"(x))
#define PIN2(x, y) asm volatile("" : "+v"(x), "+v"(y))
#define NOPD(n, x, y) asm volatile("s_nop " #n : "+v"(x), "+v"(y))
#define SMA(d, a, b) asm volatile("v_smfmac_f32_16x16x64_bf16 %0, %1, %2, %3" : "+v"(d) : "v"(a), "a"(b), "v"(idx))
#define SMV(d, a, b) asm volatile("v_smfmac_f32_16x16x64_bf16 %0, %1, %2, %3" : "+v"(d) : "v"(a), "v"(b), "v"(idx))
#define MM0(d, a, b) asm volatile("v_mfma_f32_16x16x32_bf16 %0, %1, %2, 0" : "=&v"(d) : "v"(a), "v"(b))
#define MMA(d, a, b) asm volatile("v_mfma_f32_16x16x32_bf16 %0, %1, %2, %0" : "+v"(d) : "v"(a), "v"(b))
#define M16(d, a, b) asm volatile("v_mfma_f32_16x16x16_bf16 %0, %1, %2, %0" : "+v"(d) : "v"(a), "v"(b))
#define SWP(x, y, i) do { auto r_ = __builtin_amdgcn_permlane32_swap(__float_as_uint(x[i]), __float_as_uint(y[i]), false, false); \
    x[i] = __uint_as_float(r_[0]); y[i] = __uint_as_float(r_[1]); } while (0)
#define LW(a, i) cat(lr16((a) + (i) * 2048), lr16((a) + (i) * 2048 + 1024))
#define PUBLISH(s) do { if (c == 0) { const u32x4 kmw = cat(trd(aX0 + 2048 + w * 512), trd(aX1 + 2048 + w * 512)); \
      if (!(l & 8)) st16(kmw, rw, h * 2048 + w * 512 + g * 128 + (l & 7) * 16, O_KM + (s) * 65536); \
    } else st8(vm, rw, h * 2048 + w * 512 + l * 8, O_VM + (s) * 65536); } while (0)
#define LDA(d, v, so) asm volatile("buffer_load_dwordx4 %0, %1, %2, %3 offen sc1" : "=&v"(d) : "v"(v), "s"(rw), "s"(so) : "memory")
#define EX(x) __builtin_amdgcn_exp2f(__builtin_fmaf(x, sc, mx))
  lw16(aK + w * 2048, kn0); lw16(aK + w * 2048 + 1024, kn1);
  lw16(aV, vn0); lw16(aV + 1024, vn1);
  u32x2 vm = {0u, 0u};
  const u32 vPub = h * 2048 + w * 512 + (c ? l * 8 : g * 128 + (l & 7) * 16);
  for (int s = 0;; ++s) {
    f32x4 qx = z4, qy = z4, kx = z4, ky = z4;
    PIN2(qx, qy); PIN2(kx, ky);
    Wk[4][0] = LW(aWk, 0); Wk[4][1] = LW(aWk, 1); Wk[5][0] = LW(aWk, 2); Wk[5][1] = LW(aWk, 3);
    delay<D2>();
    CYC(0);
    bool ok;
    u32 spins = 0;
#pragma unroll 1
    do {
      MEMB;
      const u32x4 ma = ld16(rw, vMem, O_MEM + s * 16384), mb = ld16(rw, vMem + 1024, O_MEM + s * 16384);
      np2 += s >= 20;
      lw16(aM, ma); lw16(aM + 1024, mb);
      ok = nosentinel(umax3(umax3(ma.x, ma.y, ma.z), umax3(mb.x, mb.y, mb.z), umax3(ma.w, mb.w, mb.w)));
    } while (!ok && ++spins < SPIN_CAP);
    if (!ok) { st4(2, rw, O_CTRL + F_TMO * 128, 0); halt(); }
    CYC(1);
    bar();
    CYC(2);
    u32x4 af[8];
#define AF(B) do { MEMB; af[B] = cat(trd(aA0 + (B) * 1024), trd(aA1 + (B) * 1024)); MEMB; } while (0)
    AF(0); AF(1); AF(2); AF(3);
    SMA(qx, af[0], Wq[0][0]); AF(4); SMA(qy, af[0], Wq[0][1]); AF(5);
    SMA(qx, af[1], Wq[1][0]); AF(6); SMA(qy, af[1], Wq[1][1]); AF(7);
    const u32x4 kmp = cat(trd(aX0 + 2048 + w * 512), trd(aX1 + 2048 + w * 512)); MEMB;
    SMA(qx, af[2], Wq[2][0]); MEMB;
    const u32 s1 = s < 63 ? s + 1 : 63;
    if (prog < s1 + 1) {
      prog = wait_flags(rw, F_KV + (s1 >> 5) * 48 + h, (s1 & 31) + 1, l == 0);
      if (!prog) halt();
      prog += s1 & 32;
    }
    LDA(kn0, vKV, O_K + s1 * 262144);
    SMA(qy, af[2], Wq[2][1]);
#pragma unroll
    for (int B = 3; B < 8; ++B) {
      SMA(qx, af[B], Wq[B][0]);
      if (B == 3 && s) { if (c) st8(vm, rw, vPub, O_VM + (s - 1) * 65536); }
      if (B == 4) LDA(kn1, vKV2, O_K + s1 * 262144);
      if (B == 5 && s) { if (!c && !(l & 8)) st16(kmp, rw, vPub, O_KM + (s - 1) * 65536); }
      if (B == 7 && s) { if (tid == 0) st4(s - 1, rw, O_CTRL + (F_KM + rr) * 128, 0); }
      if (B == 6) LDA(vn0, vKV, O_V + s1 * 262144);
      SMA(qy, af[B], Wq[B][1]);
      if (B < 7) { MEMB; Wk[6 + ((B - 3) >> 1)][(B - 3) & 1] = LW(aWk, B + 1); MEMB; }
    }
#pragma unroll
    for (int B = 4; B < 7; ++B) {
      SMV(kx, af[B], Wk[B][0]);
      if (B == 4) LDA(vn1, vKV2, O_V + s1 * 262144);
      SMV(ky, af[B], Wk[B][1]);
    }
    NOPD(1, qx, qy);
    SWP(qx, qy, 0); SWP(qx, qy, 1); PIN2(qx, qy);
    SMV(kx, af[7], Wk[7][0]);
    SWP(qx, qy, 2); SWP(qx, qy, 3); PIN2(qx, qy);
    SMV(ky, af[7], Wk[7][1]);
    qx = add4(qx, qy); PIN(qx);
    SMV(kx, af[0], Wk[0][0]);
    { u32x2 t2 = pk4(qx); PIN(t2); SMV(ky, af[0], Wk[0][1]); MEMB; lw8(aXw, t2); MEMB; }
    u32x4 qf[4], kf[4][2], km[4];
    f32x4 s0, s1v, sma, vx = z4, vy = z4;
    PIN2(vx, vy);
    kf[0][0] = lr16(aK); kf[0][1] = lr16(aK + 1024); MEMB;
    SMV(kx, af[1], Wk[1][0]); MEMB;
    SMV(ky, af[1], Wk[1][1]); MEMB;
    kf[1][0] = lr16(aK + 2048); kf[1][1] = lr16(aK + 3072); MEMB;
    SMV(kx, af[2], Wk[2][0]); MEMB;
    kf[2][0] = lr16(aK + 4096); kf[2][1] = lr16(aK + 5120); MEMB;
    SMV(ky, af[2], Wk[2][1]); MEMB;
    kf[3][0] = lr16(aK + 6144); kf[3][1] = lr16(aK + 7168); MEMB;
    SMV(kx, af[3], Wk[3][0]); SMV(ky, af[3], Wk[3][1]);
    SMA(vx, af[0], Wv[0][0]); SMA(vy, af[0], Wv[0][1]);
    NOPD(0, kx, ky);
    SWP(kx, ky, 0); SWP(kx, ky, 1); PIN2(kx, ky);
    SMA(vx, af[1], Wv[1][0]);
    SWP(kx, ky, 2); SWP(kx, ky, 3); PIN2(kx, ky);
    SMA(vy, af[1], Wv[1][1]);
    kx = add4(kx, ky);
    { u32x2 t2 = pk4(kx); PIN(t2); SMA(vx, af[2], Wv[2][0]); MEMB; lw8(aXw + 2048, t2); MEMB; }
    SMA(vy, af[2], Wv[2][1]); MEMB;
    SMA(vx, af[3], Wv[3][0]); SMA(vy, af[3], Wv[3][1]);
    bar();
    CYC(3);
    qf[0] = cat(trd(aX0), trd(aX1)); km[0] = cat(trd(aX0 + 2048), trd(aX1 + 2048)); MEMB;
    SMA(vx, af[4], Wv[4][0]); MEMB;
    qf[1] = cat(trd(aX0 + 512), trd(aX1 + 512)); km[1] = cat(trd(aX0 + 2560), trd(aX1 + 2560)); MEMB;
    SMA(vy, af[4], Wv[4][1]); MEMB;
    qf[2] = cat(trd(aX0 + 1024), trd(aX1 + 1024)); km[2] = cat(trd(aX0 + 3072), trd(aX1 + 3072)); MEMB;
    SMA(vx, af[5], Wv[5][0]); MEMB;
    qf[3] = cat(trd(aX0 + 1536), trd(aX1 + 1536)); km[3] = cat(trd(aX0 + 3584), trd(aX1 + 3584)); MEMB;
    SMA(vy, af[5], Wv[5][1]); MEMB;
    const u32x4 vc0 = lr16(aV), vc1 = lr16(aV + 1024); MEMB;
    SMA(vx, af[6], Wv[6][0]); SMA(vy, af[6], Wv[6][1]);
    MM0(s0, kf[0][0], qf[0]); MM0(s1v, kf[0][1], qf[0]); MM0(sma, km[0], qf[0]);
#pragma unroll
    for (int eb = 1; eb < 4; ++eb) { MMA(s0, kf[eb][0], qf[eb]); MMA(s1v, kf[eb][1], qf[eb]); MMA(sma, km[eb], qf[eb]); }
    SMA(vx, af[7], Wv[7][0]); MEMB;
    u32x8 Wo[4][2];
    Wo[0][0] = LW(aWo, 0); Wo[0][1] = LW(aWo, 1); MEMB;
    PIN2(s0, s1v);
    float mx = max3(max3(s0[0], s0[1], s0[2]), max3(s0[3], s1v[0], s1v[1]), max3(s1v[2], s1v[3], s1v[3]));
    PIN(mx);
    SMA(vy, af[7], Wv[7][1]); MEMB;
    Wo[1][0] = LW(aWo, 2); Wo[1][1] = LW(aWo, 3); MEMB;
    PIN(sma);
    mx = max3(max3(mx, sma[0], sma[1]), sma[2], sma[3]);
    { auto r_ = __builtin_amdgcn_permlane16_swap(__float_as_uint(mx), __float_as_uint(mx), false, false); mx = max3(__uint_as_float(r_[0]), __uint_as_float(r_[1]), __uint_as_float(r_[1])); }
    { auto r_ = __builtin_amdgcn_permlane32_swap(__float_as_uint(mx), __float_as_uint(mx), false, false); mx = max3(__uint_as_float(r_[0]), __uint_as_float(r_[1]), __uint_as_float(r_[1])) * -sc; }
    asm volatile("" : "+v"(mx), "+v"(vx), "+v"(vy));
#pragma unroll
    for (int i = 0; i < 4; ++i) {
      s0[i] = EX(s0[i]); s1v[i] = EX(s1v[i]);
      SWP(vx, vy, i); vx[i] = fadd(vx[i], vy[i]);
    }
    u32x4 pf = cat(pk4(s0), pk4(s1v));
    vm = pk4(vx);
    asm volatile("s_nop 0" : "+v"(pf), "+v"(vm));
    f32x4 ox, oy, su;
    MM0(su, ones, pf);
    sma[0] = EX(sma[0]); sma[1] = EX(sma[1]); PIN(sma);
    MM0(ox, vc0, pf);
    sma[2] = EX(sma[2]); sma[3] = EX(sma[3]); PIN(sma);
    MM0(oy, vc1, pf);
    u32x2 pm = pk4(sma), pmx, pmy;
    pmx.x = g < 2 ? pm.x : 0u; pmx.y = g < 2 ? pm.y : 0u; pmy.x = g < 2 ? 0u : pm.x; pmy.y = g < 2 ? 0u : pm.y;
    asm volatile("s_nop 0" : "+v"(pmx), "+v"(pmy));
    M16(su, ones2, pmx); M16(ox, vm, pmx); M16(oy, vm, pmy); MEMB;
    Wo[2][0] = LW(aWo, 4); Wo[2][1] = LW(aWo, 5); Wo[3][0] = LW(aWo, 6); Wo[3][1] = LW(aWo, 7);
    asm volatile("" : "+v"(su) :: "memory");
    float inv = __builtin_amdgcn_rcpf(su[0]);
    PIN(inv); PIN2(ox, oy);
    CYC(4);
    if (s == 63) { asm volatile("s_waitcnt vmcnt(0)" :: "v"(kn0), "v"(kn1), "v"(vn0), "v"(vn1) : "memory"); PUBLISH(63); break; }
    {
      lw16(aO + w * 1024, cat(pk4(mul4(ox, inv)), pk4(mul4(oy, inv)))); MEMB;
      f32x4 d0 = z4, d1 = z4;
      PIN2(d0, d1);
      if (s >= 61) waitvm<0>();
      bar();
      CYC(5);
      u32x4 of[4];
#pragma unroll
      for (int eb = 0; eb < 4; ++eb) of[eb] = lr16(aO + eb * 1024);
      MEMB;
#pragma unroll
      for (int eb = 0; eb < 4; ++eb) { SMV(d0, of[eb], Wo[eb][0]); SMV(d1, of[eb], Wo[eb][1]); }
      asm volatile("s_nop 6" : "+v"(d0));
      u32x2 o0 = pk4(d0); PIN(o0); PIN(d1);
      u32x2 o1 = pk4(d1);
      st16(u32x4{nosent(o0.x), nosent(o0.y), nosent(o1.x), nosent(o1.y)}, rw, vPst, O_RING + (s & 1) * 524288);
      MEMB;
      CYC(6);
      asm volatile("s_waitcnt vmcnt(1)\n\tds_write_b128 %4, %0\n\tds_write_b128 %4, %1 offset:1024\n\tds_write_b128 %5, %2\n\tds_write_b128 %5, %3 offset:1024"
                   :: "v"(kn0), "v"(kn1), "v"(vn0), "v"(vn1), "v"(aK + w * 2048), "v"(aV) : "memory");
    }
    if (s >= 61 && tid == 0) st4(s, rw, O_CTRL + (F_KM + rr) * 128, 0);
    if (r == 0) STAMP(16 + s);
    if (s < 3) STAMP(728 + s * 64 + r);
    if (s < 62 && prog < s + 3) {
      const u32 v = __builtin_amdgcn_readfirstlane(ld4(rw, O_CTRL + (F_KV + ((s + 2) >> 5) * 48 + h) * 128, 0));
      if (v && prog >= ((s + 2) & 32)) prog = v + ((s + 2) & 32);
    } else delay<D1>();
    CYC(7);
    f32x4 t;
    spins = 0;
#pragma unroll 1
    do {
      MEMB;
      const u32x4 pp = ld16(rw, vPld, O_RING + (s & 1) * 524288);
      np1 += s >= 20;
      MM0(t, sel, pp);
      ok = nosentinel(umax3(umax3(pp.x, pp.y, pp.z), pp.w, pp.w));
    } while (!ok && ++spins < SPIN_CAP);
    if (!ok) { st4(3, rw, O_CTRL + F_TMO * 128, 0); halt(); }
    CYC(8);
#define RA(i, n) "v_add_f32_dpp %" #i ", %" #i ", %" #i " row_ror:" #n " row_mask:0xf bank_mask:0xf\n\t"
    asm volatile("s_nop 3\n\t" RA(0, 2) RA(1, 2) RA(2, 2) RA(3, 2) RA(0, 4) RA(1, 4) RA(2, 4) RA(3, 4) RA(0, 8) RA(1, 8) RA(2, 8) RA(3, 8)
                 : "+v"(t[0]), "+v"(t[1]), "+v"(t[2]), "+v"(t[3]));
    u32x2 mo = pk4(t);
    if ((l & 0x2e) == 0) st8(u32x2{nosent(mo.x), nosent(mo.y)}, rw, vMst, O_MEM + (s + 1) * 16384);
    MEMB;
    CYC(9);
    st16(u32x4{~0u, ~0u, ~0u, ~0u}, rw, vPld, O_RING + (s & 1) * 524288);
  }
  waitvm<0>();
  bar();
  if (r == 0) { STAMP(3); STAMP(79); PROFV(8, np1); PROFV(9, np2); PROFV(10, side); }
  if (tid == 0) st4(64, rw, O_CTRL + (F_KM + rr) * 128, 0);
}

template <int ROWS, int RB> DEV void stage_w(const char* g, size_t rowstride, int l, u32 dst) {
  constexpr int LPR = RB / 16, RPI = 64 / LPR;
  const char* a = g + (size_t)(l / LPR) * rowstride + (l % LPR) * 16;
#pragma unroll
  for (int i = 0; i < ROWS / RPI; ++i) dma16(a + (size_t)i * RPI * rowstride, dst + i * 1024);
}
DEV bool wg_wait(rsrc_t rw, LAS char* sm, int w, u32 flag, u32 target, bool active) {
  LAS u32* okw = (LAS u32*)(sm + 155584);
  if (w == 0) *okw = wait_flags(rw, flag, target, active);
  bar();
  const u32 ok = *okw;
  bar();
  return ok != 0;
}

DEV void phase_ln1(const Params& p, rsrc_t rw, LAS char* sm, int wk) {
  const int tid = threadIdx.x, l = tid & 63, w = tid >> 6;
  const float* mod = (const float*)(align_ws(p.wsraw) + O_MOD);
  f32x4 sa0 = *(const f32x4*)(mod + l * 8), sa1 = *(const f32x4*)(mod + l * 8 + 4);
  f32x4 ca0 = *(const f32x4*)(mod + 512 + l * 8), ca1 = *(const f32x4*)(mod + 512 + l * 8 + 4);
  u32x4 xv[4];
#pragma unroll
  for (int i = 0; i < 4; ++i) xv[i] = *(const u32x4*)(p.x + (size_t)(wk * 16 + w * 4 + i) * 512 + l * 8);
#pragma unroll
  for (int i = 0; i < 4; ++i) {
    float v[8], s = 0.f, q = 0.f;
#pragma unroll
    for (int j = 0; j < 4; ++j) { v[2 * j] = lo(xv[i][j]); v[2 * j + 1] = hi(xv[i][j]); s += v[2 * j] + v[2 * j + 1]; }
    s = sw32(sw16(ror_add<8>(ror_add<4>(ror_add<2>(ror_add<1>(s)))), false), false) * (1.f / 512);
#pragma unroll
    for (int j = 0; j < 8; ++j) { v[j] -= s; q += v[j] * v[j]; }
    q = sw32(sw16(ror_add<8>(ror_add<4>(ror_add<2>(ror_add<1>(q)))), false), false) * (1.f / 512);
    const float rs = rsqrtf(q + 1e-5f);
    u32x4 o;
#pragma unroll
    for (int j = 0; j < 4; ++j) {
      const float c0 = j < 2 ? ca0[2 * j] : ca1[2 * j - 4], c1 = j < 2 ? ca0[2 * j + 1] : ca1[2 * j - 3];
      const float a0 = j < 2 ? sa0[2 * j] : sa1[2 * j - 4], a1 = j < 2 ? sa0[2 * j + 1] : sa1[2 * j - 3];
      o[j] = pk(v[2 * j] * rs * (1.f + c0) + a0, v[2 * j + 1] * rs * (1.f + c1) + a1);
    }
    lw16(sm + 131072 + l * 256 + (((w * 4 + i) ^ l) & 15) * 16, o);
  }
  bar();
#pragma unroll
  for (int i = 0; i < 4; ++i) {
    const int ch = i * 256 + tid;
    st16(lr16(sm + 131072 + (ch >> 4) * 256 + ((ch ^ (ch >> 4)) & 15) * 16), rw, wk * 16384 + ch * 16, O_H);
  }
  waitvm<0>();
  bar();
  if (tid == 0) add_flag(align_ws(p.wsraw), F_H + (wk >> 1));
}

template <bool ISV> DEV bool qkv_loop(rsrc_t rw, LAS char* sm, const u32x4 (&W)[16][4], int rg, u32 obase, u32 flag) {
  const int tid = threadIdx.x, l = tid & 63, w = tid >> 6;
  const f32x4 z4 = {0.f, 0.f, 0.f, 0.f};
  u32x4 pf[8];
#pragma unroll
  for (int j = 0; j < 8; ++j) pf[j] = ld16(rw, w * 8192 + j * 1024 + l * 16, O_H + rg * 32 * 32768);
#pragma unroll
  for (int j = 0; j < 8; ++j) lw16(sm + w * 8192 + j * 1024 + l * 16, pf[j]);
  bar();
  for (int i = 0; i < 32; ++i) {
    const int seg = rg * 32 + i;
    if (i < 31) {
#pragma unroll
      for (int j = 0; j < 8; ++j) pf[j] = ld16(rw, w * 8192 + j * 1024 + l * 16, O_H + (seg + 1) * 32768);
    }
    LAS char* a = sm + (i & 1) * 32768 + l * 16;
    f32x4 acc[2][4];
#pragma unroll
    for (int t = 0; t < 4; ++t) { acc[0][t] = z4; acc[1][t] = z4; }
#pragma unroll
    for (int ks = 0; ks < 16; ++ks) {
      const u32x4 h0 = lr16(a + ks * 1024), h1 = lr16(a + 16384 + ks * 1024);
#pragma unroll
      for (int t = 0; t < 4; ++t) {
        acc[0][t] = ISV ? mma(h0, W[ks][t], acc[0][t]) : mma(W[ks][t], h0, acc[0][t]);
        acc[1][t] = ISV ? mma(h1, W[ks][t], acc[1][t]) : mma(W[ks][t], h1, acc[1][t]);
      }
    }
#pragma unroll
    for (int t = 0; t < 4; ++t) {
      const u32x4 o = ISV ? cat(pk4(acc[0][t]), pk4(acc[1][t])) : cat(pk4(acc[t & 1][t & 2]), pk4(acc[t & 1][(t & 2) + 1]));
      st16(o, rw, t * 1024 + l * 16, obase + seg * 262144);
    }
    if (i < 3 || i == 31) waitvm<0>(); else waitvm<4>();
    if (i < 31) {
#pragma unroll
      for (int j = 0; j < 8; ++j) lw16(sm + ((i + 1) & 1) * 32768 + w * 8192 + j * 1024 + l * 16, pf[j]);
    }
    bar();
    if (tid == 0) st4(i < 3 || i == 31 ? i + 1 : i, rw, O_CTRL + flag * 128, 0);
  }
  return true;
}

#define PREF 1
#define RG1_AFTER 1
DEV bool phase_qkv(const Params& p, rsrc_t rw, LAS char* sm, int wk) {
  const int tid = threadIdx.x, l = tid & 63, w = __builtin_amdgcn_readfirstlane(tid >> 6);
  const int cb = wk % 48, rg = wk / 48, g = l >> 4, q4 = (l >> 2) & 3, p4 = l & 3;
  const bool isv = cb < 32 && w >= 2;
  const int head = cb < 32 ? cb : 2 * (cb - 32) + (w >> 1);
  const __bf16* M = cb >= 32 ? p.wq : (isv ? p.wv : p.wk);
  const char* gsrc = (const char*)(M + head * 128 + (w & 1) * 64);
  const u32 dst = (u32)(size_t)sm + w * 32768;
  if (wk >= 128 && PREF) {
    const int i = (wk - 128) >> 2, hh = (wk & 3) + 4 * (i >> 1), hf = i & 1;
    const size_t o = (size_t)(hf * 256 + w * 64 + (l >> 4)) * 8192 + hh * 256 + (l & 15) * 16;
    const char* go = (const char*)p.wo + (size_t)(hh * 128 + w * 32 + (l >> 5)) * 1024 + hf * 512 + (l & 31) * 16;
#pragma unroll 4
    for (int j = 0; j < 16; ++j) dma16((const char*)p.wv + o + (size_t)j * 32768, dst + j * 1024);
#pragma unroll 4
    for (int j = 0; j < 16; ++j) dma16((const char*)p.wk + o + (size_t)j * 32768, dst + j * 1024);
#pragma unroll 4
    for (int j = 0; j < 16; ++j) dma16(go + j * 2048, dst + j * 1024);
    waitvm<0>();
  }
  if (rg) {
    if (wk < 176) phase_ln1(p, rw, sm, wk - 48);
    if (rg > 1) return true;
    if (!wg_wait(rw, sm, w, F_KV + cb, RG1_AFTER, l == 0)) return false;
  }
  stage_w<128, 128>(gsrc, 8192, l, dst); stage_w<128, 128>(gsrc + 128 * 8192, 8192, l, dst + 16384);
  u32x4 W[16][4];
  LAS char* a = sm + w * 32768 + (8 * g + q4) * 128 + (isv ? p4 * 8 : p4 * 16);
  const int tstep = isv ? 32 : 8;
#pragma unroll
  for (int c = 0; c < 4; ++c) {
    if (c < 3) waitvm<16>(); else waitvm<0>();
#pragma unroll
    for (int k = 0; k < 4; ++k)
#pragma unroll
      for (int t = 0; t < 4; ++t) {
        LAS char* q = a + (c & 1) * 16384 + k * 4096 + (t >> 1) * 64 + (t & 1) * tstep;
        W[c * 4 + k][t] = cat(trd(q), trd(q + 512));
      }
    waitlds();
    if (c < 2) stage_w<128, 128>(gsrc + (size_t)(c + 2) * 128 * 8192, 8192, l, dst + (c & 1) * 16384);
  }
  if (wk == 0) STAMP(4);
  if (!wg_wait(rw, sm, w, F_H + rg * 32 + l, 2, l < 32)) return false;
  if (wk == 0) STAMP(5);
  const u32 obase = (cb >= 32 ? O_Q : (isv ? O_V : O_K)) + head * 8192 + (w & 1) * 4096;
  if (isv) qkv_loop<true>(rw, sm, W, rg, obase, F_KV + rg * 48 + cb);
  else qkv_loop<false>(rw, sm, W, rg, obase, F_KV + rg * 48 + cb);
  return true;
}

DEV void role_attn(const Params& p, rsrc_t rw, LAS char* sm, int set, int hp) {
  const int tid = threadIdx.x, l = tid & 63, w = __builtin_amdgcn_readfirstlane(tid >> 6);
  const int g = l >> 4, n = l & 15, q4 = (l >> 2) & 3, p4 = l & 3;
  const f32x4 z4 = {0.f, 0.f, 0.f, 0.f};
  const char* gsrc = (const char*)(p.wo + (size_t)hp * 256 * 512 + w * 128);
  const u32 dst = (u32)(size_t)sm + w * 32768;
  u32x4 W[8][8];
  stage_w<64, 256>(gsrc, 1024, l, dst); stage_w<64, 256>(gsrc + 64 * 1024, 1024, l, dst + 16384);
  LAS char* a = sm + w * 32768 + (4 * g + q4) * 256 + p4 * 16;
#pragma unroll
  for (int c = 0; c < 4; ++c) {
    if (c < 3) waitvm<16>(); else waitvm<0>();
#pragma unroll
    for (int k = 0; k < 2; ++k)
#pragma unroll
      for (int t = 0; t < 8; ++t) {
        LAS char* q = a + (c & 1) * 16384 + k * 8192 + (t >> 1) * 64 + (t & 1) * 8;
        W[c * 2 + k][t] = cat(trd(q), trd(q + 4096));
      }
    waitlds();
    if (c < 2) stage_w<64, 256>(gsrc + (c + 2) * 64 * 1024, 1024, l, dst + (c & 1) * 16384);
  }
  const int hd = hp * 2 + (w >> 1), qt = w & 1;
  const float sc = 0.08838834764831845f * 1.4426950408889634f;
  bar();
  for (int s = set; s < 64; s += 3) {
    {
      const u32 rgb = F_KV + (s >> 5) * 48;
      if (!wait_flags(rw, l < 1 ? rgb + hd : rgb + 32 + hp, (s & 31) + 1, l < 2)) halt();
    }
    const u32 vo = hd * 8192 + l * 16, so = s * 262144;
    u32x4 kf[4][2], qf[4], km[4], vf[4][2]; u32x2 vm[4];
#pragma unroll
    for (int eb = 0; eb < 4; ++eb) {
      qf[eb] = ld16(rw, vo + eb * 2048 + qt * 1024, O_Q + so);
      kf[eb][0] = ld16(rw, vo + eb * 2048, O_K + so); kf[eb][1] = ld16(rw, vo + eb * 2048 + 1024, O_K + so);
    }
#pragma unroll
    for (int eb = 0; eb < 4; ++eb) { vf[eb][0] = ld16(rw, vo + eb * 2048, O_V + so); vf[eb][1] = ld16(rw, vo + eb * 2048 + 1024, O_V + so); }
    f32x4 s0 = z4, s1 = z4, sm_ = z4;
#pragma unroll
    for (int eb = 0; eb < 4; ++eb) { s0 = mma(kf[eb][0], qf[eb], s0); s1 = mma(kf[eb][1], qf[eb], s1); }
    MEMB;
    if (!wait_flags(rw, F_KM + hd * 2 + l, s + 1, l < 2, s > 61)) halt();
    if (s == 63 && hp == 0) STAMP(930);
#pragma unroll
    for (int eb = 0; eb < 4; ++eb) km[eb] = ld16(rw, hd * 2048 + eb * 512 + g * 128 + (l & 7) * 16, O_KM + s * 65536);
#pragma unroll
    for (int eb = 0; eb < 4; ++eb) vm[eb] = ld8(rw, hd * 2048 + eb * 512 + l * 8, O_VM + s * 65536);
#pragma unroll
    for (int eb = 0; eb < 4; ++eb) sm_ = mma(km[eb], qf[eb], sm_);
    float mx = -3.0e38f;
#pragma unroll
    for (int i = 0; i < 4; ++i) {
      s0[i] = (4 * g + i <= 16 * qt + n) ? s0[i] * sc : -1.4426950408889634e9f;
      s1[i] = (16 + 4 * g + i <= 16 * qt + n) ? s1[i] * sc : -1.4426950408889634e9f;
      sm_[i] *= sc;
      mx = fmaxf(mx, fmaxf(fmaxf(s0[i], s1[i]), sm_[i]));
    }
    mx = sw32(sw16(mx, true), true);
    float sum = 0.f, summ = 0.f;
#pragma unroll
    for (int i = 0; i < 4; ++i) {
      s0[i] = __builtin_amdgcn_exp2f(s0[i] - mx); s1[i] = __builtin_amdgcn_exp2f(s1[i] - mx); sm_[i] = __builtin_amdgcn_exp2f(sm_[i] - mx);
      sum += s0[i] + s1[i]; summ += sm_[i];
    }
    const float inv = __builtin_amdgcn_rcpf(sw32(sw16(sum + 0.5f * summ, false), false));
    const u32x4 pf = cat(pk4(s0), pk4(s1));
    const u32x2 pm = pk4(sm_), zz = {0u, 0u}, pmx = g < 2 ? pm : zz, pmy = g < 2 ? zz : pm;
#pragma unroll
    for (int eb = 0; eb < 4; ++eb) {
      const f32x4 ox = mma16(vm[eb], pmx, mma(vf[eb][0], pf, z4)), oy = mma16(vm[eb], pmy, mma(vf[eb][1], pf, z4));
      lw16(sm + (qt * 8 + (w >> 1) * 4 + eb) * 1024 + l * 16, cat(pk4(ox * inv), pk4(oy * inv)));
    }
    bar();
#pragma unroll
    for (int t2 = 0; t2 < 2; ++t2) {
      f32x4 acc[8];
#pragma unroll
      for (int t = 0; t < 8; ++t) acc[t] = z4;
#pragma unroll
      for (int ks = 0; ks < 8; ++ks) {
        const u32x4 of = lr16(sm + (t2 * 8 + ks) * 1024 + l * 16);
#pragma unroll
        for (int t = 0; t < 8; ++t) acc[t] = mma(W[ks][t], of, acc[t]);
      }
#pragma unroll
      for (int u = 0; u < 4; ++u)
        st16(cat(pk4(acc[2 * u]), pk4(acc[2 * u + 1])), rw, hp * 32768 + t2 * 16384 + (w * 4 + u) * 1024 + l * 16, O_P1 + s * 524288);
    }
    waitvm<0>();
    bar();
    if (tid == 0) add_flag(align_ws(p.wsraw), F_P1 + s);
    if (s == 63 && hp == 0) STAMP(931);
  }
}

DEV void role_norm(const Params& p, rsrc_t rw, LAS char* sm, int set, int j) {
  const int tid = threadIdx.x, l = tid & 63, w = __builtin_amdgcn_readfirstlane(tid >> 6), t4 = tid & 3, pc = tid >> 2;
  const float* mod = (const float*)(align_ws(p.wsraw) + O_MOD) + pc * 8;
  float ga[8], sb[8], cb[8];
#pragma unroll
  for (int i = 0; i < 8; ++i) { ga[i] = mod[1024 + i]; sb[i] = mod[1536 + i]; cb[i] = 1.f + mod[2048 + i]; }
  LAS float* red = (LAS float*)sm;
  const u32 vo = (j >> 2) * 16384 + pc * 256 + ((4 * j + t4) & 15) * 16;
  for (int s = set; s < 64; s += 3) {
    const u32x4 xv = *(const u32x4*)(p.x + (size_t)(s * 32 + 4 * j + t4) * 512 + pc * 8);
    if (!wait_flags(rw, F_P1 + s, 16, l < 1, s > 61)) halt();
    if (s == 63 && j == 0) STAMP(932);
    u32x4 pp[16];
#pragma unroll
    for (int i = 0; i < 16; ++i) pp[i] = ld16(rw, vo + i * 32768, O_P1 + s * 524288);
    float v[8] = {0, 0, 0, 0, 0, 0, 0, 0};
#pragma unroll
    for (int i = 0; i < 16; ++i)
#pragma unroll
      for (int e = 0; e < 4; ++e) { v[2 * e] += lo(pp[i][e]); v[2 * e + 1] += hi(pp[i][e]); }
    u32x4 x1; float su = 0.f, q = 0.f;
#pragma unroll
    for (int e = 0; e < 4; ++e) {
      x1[e] = pk(lo(xv[e]) + ga[2 * e] * rb(v[2 * e]), hi(xv[e]) + ga[2 * e + 1] * rb(v[2 * e + 1]));
      v[2 * e] = lo(x1[e]); v[2 * e + 1] = hi(x1[e]); su += v[2 * e] + v[2 * e + 1];
    }
    st16(x1, rw, vo, O_X1 + s * 32768);
    su = sw32(sw16(ror_add<8>(ror_add<4>(su)), false), false);
    if (l < 4) red[w * 4 + l] = su;
    bar();
    su = (red[t4] + red[4 + t4] + red[8 + t4] + red[12 + t4]) * (1.f / 512);
#pragma unroll
    for (int e = 0; e < 8; ++e) { v[e] -= su; q += v[e] * v[e]; }
    q = sw32(sw16(ror_add<8>(ror_add<4>(q)), false), false);
    if (l < 4) red[16 + w * 4 + l] = q;
    bar();
    const float rs = rsqrtf((red[16 + t4] + red[20 + t4] + red[24 + t4] + red[28 + t4]) * (1.f / 512) + 1e-5f);
    u32x4 o;
#pragma unroll
    for (int e = 0; e < 4; ++e) o[e] = pk(v[2 * e] * rs * cb[2 * e] + sb[2 * e], v[2 * e + 1] * rs * cb[2 * e + 1] + sb[2 * e + 1]);
    st16(o, rw, vo, O_H2 + s * 32768);
    waitvm<0>();
    bar();
    if (tid == 0) add_flag(align_ws(p.wsraw), F_H2 + s);
    if (s == 63 && j == 0) STAMP(933);
  }
}

DEV float gelu(float x) {
  const float u = x + 0.044715f * x * x * x;
  return x * __builtin_amdgcn_rcpf(1.f + __builtin_amdgcn_exp2f(u * (-2.f * 0.7978845608028654f * 1.4426950408889634f)));
}
DEV void role_mlp(const Params& p, rsrc_t rw, LAS char* sm, int set, int fs) {
  const int tid = threadIdx.x, l = tid & 63, w = __builtin_amdgcn_readfirstlane(tid >> 6);
  const int g = l >> 4, q4 = (l >> 2) & 3, p4 = l & 3;
  const f32x4 z4 = {0.f, 0.f, 0.f, 0.f};
  const bool pad = fs == 10 && w == 3;
  const int f0 = pad ? 0 : fs * 128 + w * 32;
  const u32 dst = (u32)(size_t)sm + w * 32768;
  u32x4 W1[16][2], W2[4][8];
  const char* g1 = (const char*)(p.w1 + f0);
  const char* g2 = (const char*)(p.w2 + (size_t)fs * 128 * 512 + w * 128);
  stage_w<256, 64>(g1, 2752, l, dst); stage_w<256, 64>(g1 + 256 * 2752, 2752, l, dst + 16384);
#pragma unroll
  for (int c = 0; c < 2; ++c) {
    waitvm<16>();
    LAS char* a = sm + w * 32768 + c * 16384 + (8 * g + q4) * 64 + p4 * 16;
#pragma unroll
    for (int k = 0; k < 8; ++k)
#pragma unroll
      for (int t = 0; t < 2; ++t) W1[c * 8 + k][t] = cat(trd(a + k * 2048 + t * 8), trd(a + k * 2048 + t * 8 + 256));
    waitlds();
    stage_w<32, 256>(g2 + c * 64 * 1024, 1024, l, dst + c * 16384);
    stage_w<32, 256>(g2 + ((c == 1 && fs == 10) ? 0 : (c * 64 + 32) * 1024), 1024, l, dst + c * 16384 + 8192);
  }
#pragma unroll
  for (int c = 0; c < 2; ++c) {
    if (c == 0) waitvm<16>(); else waitvm<0>();
    LAS char* a = sm + w * 32768 + c * 16384 + (8 * g + q4) * 256 + p4 * 16;
#pragma unroll
    for (int k = 0; k < 2; ++k)
#pragma unroll
      for (int t = 0; t < 8; ++t) {
        LAS char* q = a + k * 8192 + (t >> 1) * 64 + (t & 1) * 8;
        W2[c * 2 + k][t] = cat(trd(q), trd(q + 1024));
      }
  }
  if (fs == 10) {
#pragma unroll
    for (int t = 0; t < 8; ++t) W2[3][t] = u32x4{0u, 0u, 0u, 0u};
  }
  const u32x4 b1v = *(const u32x4*)(p.b1 + f0 + g * 8);
  bar();
  for (int s = set; s < 64; s += 3) {
    if (!wait_flags(rw, F_H2 + s, 8, l < 1, s > 61)) halt();
    if (s == 63 && fs == 0) STAMP(934);
    u32x4 pf[8];
#pragma unroll
    for (int j = 0; j < 8; ++j) pf[j] = ld16(rw, w * 8192 + j * 1024 + l * 16, O_H2 + s * 32768);
#pragma unroll
    for (int j = 0; j < 8; ++j) lw16(sm + w * 8192 + j * 1024 + l * 16, pf[j]);
    bar();
#pragma unroll
    for (int t2 = 0; t2 < 2; ++t2) {
      f32x4 a0 = {lo(b1v[0]), hi(b1v[0]), lo(b1v[1]), hi(b1v[1])}, a1 = {lo(b1v[2]), hi(b1v[2]), lo(b1v[3]), hi(b1v[3])};
#pragma unroll
      for (int ks = 0; ks < 16; ++ks) {
        const u32x4 hf = lr16(sm + t2 * 16384 + ks * 1024 + l * 16);
        a0 = mma(W1[ks][0], hf, a0); a1 = mma(W1[ks][1], hf, a1);
      }
#pragma unroll
      for (int i = 0; i < 4; ++i) { a0[i] = pad ? 0.f : gelu(rb(a0[i])); a1[i] = pad ? 0.f : gelu(rb(a1[i])); }
      lw16(sm + 32768 + (t2 * 4 + w) * 1024 + l * 16, cat(pk4(a0), pk4(a1)));
    }
    bar();
#pragma unroll
    for (int t2 = 0; t2 < 2; ++t2) {
      f32x4 acc[8];
#pragma unroll
      for (int t = 0; t < 8; ++t) acc[t] = z4;
#pragma unroll
      for (int ks = 0; ks < 4; ++ks) {
        const u32x4 gf = lr16(sm + 32768 + (t2 * 4 + ks) * 1024 + l * 16);
#pragma unroll
        for (int t = 0; t < 8; ++t) acc[t] = mma(W2[ks][t], gf, acc[t]);
      }
#pragma unroll
      for (int u = 0; u < 4; ++u)
        st16(cat(pk4(acc[2 * u]), pk4(acc[2 * u + 1])), rw, fs * 32768 + t2 * 16384 + (w * 4 + u) * 1024 + l * 16, O_P2 + s * 360448);
    }
    waitvm<0>();
    bar();
    if (tid == 0) add_flag(align_ws(p.wsraw), F_P2 + s);
    if (s == 63 && fs == 0) STAMP(935);
  }
}

DEV void role_out(const Params& p, rsrc_t rw, LAS char* sm, int set, int j) {
  const int tid = threadIdx.x, l = tid & 63, w = __builtin_amdgcn_readfirstlane(tid >> 6), t4 = tid & 3, pc = tid >> 2;
  const float* mod = (const float*)(align_ws(p.wsraw) + O_MOD) + pc * 8;
  float gb[8];
#pragma unroll
  for (int i = 0; i < 8; ++i) gb[i] = mod[2560 + i];
  const u32x4 b2v = *(const u32x4*)(p.b2 + pc * 8);
  const u32 vo = (j >> 2) * 16384 + pc * 256 + ((4 * j + t4) & 15) * 16;
  for (int s = set; s < 64; s += 3) {
    if (!wait_flags(rw, F_P2 + s, 11, l < 1, s > 61)) halt();
    if (s == 63 && j == 0) STAMP(936);
    u32x4 pp[11];
#pragma unroll
    for (int i = 0; i < 11; ++i) pp[i] = ld16(rw, vo + i * 32768, O_P2 + s * 360448);
    const u32x4 x1 = ld16(rw, vo, O_X1 + s * 32768);
    u32x4 o;
#pragma unroll
    for (int e = 0; e < 4; ++e) {
      float a = 0.f, b = 0.f;
#pragma unroll
      for (int i = 0; i < 11; ++i) { a += lo(pp[i][e]); b += hi(pp[i][e]); }
      o[e] = pk(lo(x1[e]) + gb[2 * e] * rb(rb(a) + lo(b2v[e])), hi(x1[e]) + gb[2 * e + 1] * rb(rb(b) + hi(b2v[e])));
    }
    *(u32x4*)(p.out + (size_t)(s * 32 + 4 * j + t4) * 512 + pc * 8) = o;
  }
}

__global__ __launch_bounds__(256, 1) void main_kernel(Params p) {
  __shared__ __attribute__((aligned(16))) char smem[L_END];
  LAS char* sm = (LAS char*)smem;
  const int b = blockIdx.x, i4 = (b >> 3) * 4 + (b & 3);
  if ((b & 4) == 0 && b < 128) { role_recur(p, sm, i4); return; }
  const int wk = (b & 4) ? i4 : 64 + i4;
  rsrc_t rw = mkrsrc(align_ws(p.wsraw), WS_END);
  if (!phase_qkv(p, rw, sm, wk)) return;
  STAMP(128 + wk);
  if (wk < 48) role_attn(p, rw, sm, wk / 16, wk % 16);
  else if (wk < 81) role_mlp(p, rw, sm, (wk - 48) / 11, (wk - 48) % 11);
  else if (wk < 105) role_norm(p, rw, sm, (wk - 81) / 8, (wk - 81) % 8);
  else if (wk < 129) role_out(p, rw, sm, (wk - 105) / 8, (wk - 105) % 8);
  STAMP(384 + wk);
}

extern "C" void kernel_launch(void* const* d_in, const int* in_sizes, int n_in, void* d_out, int out_size, void* d_ws,
                              size_t ws_size, hipStream_t stream) {
  Params p;
  p.x = (const __bf16*)d_in[0]; p.mem0 = (const __bf16*)d_in[1]; p.wq = (const __bf16*)d_in[2]; p.wk = (const __bf16*)d_in[3];
  p.wv = (const __bf16*)d_in[4]; p.wo = (const __bf16*)d_in[5]; p.w1 = (const __bf16*)d_in[8]; p.b1 = (const __bf16*)d_in[9];
  p.w2 = (const __bf16*)d_in[10]; p.b2 = (const __bf16*)d_in[11]; p.c = (const __bf16*)d_in[12]; p.wmod = (const __bf16*)d_in[13];
  p.out = (__bf16*)d_out;
  p.wsraw = (char*)d_ws;
  prologue_kernel<<<256, 256, 0, stream>>>(p);
  main_kernel<<<256, 256, 0, stream>>>(p);
}
```

```cpp
#include <hip/hip_runtime.h>
#include <hip/hip_bf16.h>

typedef unsigned u32;
typedef unsigned u32x2 __attribute__((ext_vector_type(2)));
typedef unsigned u32x4 __attribute__((ext_vector_type(4)));
typedef unsigned u32x8 __attribute__((ext_vector_type(8)));
typedef float f32x2 __attribute__((ext_vector_type(2)));
typedef float f32x4 __attribute__((ext_vector_type(4)));
typedef short s16x4 __attribute__((ext_vector_type(4)));
typedef __bf16 bf2 __attribute__((ext_vector_type(2)));
typedef __bf16 bfv8 __attribute__((ext_vector_type(8)));
typedef __bf16 bf16v __attribute__((ext_vector_type(16)));
typedef __amdgpu_buffer_rsrc_t rsrc_t;
#define LAS __attribute__((address_space(3)))
#define DEV __device__ __forceinline__
constexpr int SC1 = 16;
constexpr u32 SPIN_CAP = 400000;

constexpr u32 MB = 1u << 20;
constexpr u32 O_MEM = 0;
constexpr u32 O_RING = 1 * MB;
constexpr u32 O_CTRL = 2 * MB;
constexpr u32 O_MOD = 2 * MB + 131072;
constexpr u32 O_H = 4 * MB, O_X1 = 6 * MB, O_H2 = 8 * MB, O_KM = 10 * MB, O_VM = 14 * MB;
constexpr u32 O_Q = 18 * MB, O_K = 34 * MB, O_V = 50 * MB, O_P1 = 66 * MB, O_P2 = 98 * MB, WS_END = 120 * MB;
constexpr u32 F_TMO = 0, F_SIDE = 1, F_H = 2, F_KV = F_H + 64, F_KM = F_KV + 192, F_P1 = F_KM + 64, F_H2 = F_P1 + 64,
              F_P2 = F_H2 + 64, F_END = F_P2 + 64;

struct Params {
  const __bf16 *x, *mem0, *wq, *wk, *wv, *wo, *w1, *b1, *w2, *b2, *c, *wmod;
  __bf16* out;
  char* wsraw;
};

DEV char* align_ws(char* p) { return p + ((0 - (size_t)p) & (2 * MB - 1)); }
DEV rsrc_t mkrsrc(const void* p, u32 n) { return __builtin_amdgcn_make_buffer_rsrc((void*)p, 0, n, 0x00020000); }
DEV u32x4 ld16(rsrc_t r, u32 v, u32 s) { return __builtin_amdgcn_raw_buffer_load_b128(r, v, s, SC1); }
DEV u32x2 ld8(rsrc_t r, u32 v, u32 s) { return __builtin_amdgcn_raw_buffer_load_b64(r, v, s, SC1); }
DEV u32 ld4(rsrc_t r, u32 v, u32 s) { return __builtin_amdgcn_raw_buffer_load_b32(r, v, s, SC1); }
DEV void st16(u32x4 d, rsrc_t r, u32 v, u32 s) { __builtin_amdgcn_raw_buffer_store_b128(d, r, v, s, SC1); }
DEV void st8(u32x2 d, rsrc_t r, u32 v, u32 s) { __builtin_amdgcn_raw_buffer_store_b64(d, r, v, s, SC1); }
DEV void st4(u32 d, rsrc_t r, u32 v, u32 s) { __builtin_amdgcn_raw_buffer_store_b32(d, r, v, s, SC1); }
DEV u32 pk(float a, float b) { f32x2 v = {a, b}; return __builtin_bit_cast(u32, __builtin_convertvector(v, bf2)); }
DEV float lo(u32 v) { return __uint_as_float(v << 16); }
DEV float hi(u32 v) { return __uint_as_float(v & 0xffff0000u); }
DEV float rb(float a) { return lo(pk(a, a)); }
DEV u32x2 trd(LAS char* p) { return __builtin_bit_cast(u32x2, __builtin_amdgcn_ds_read_tr16_b64_v4i16((LAS s16x4*)p)); }
DEV u32x4 lr16(LAS char* p) { return *(LAS u32x4*)p; }
DEV void lw16(LAS char* p, u32x4 v) { *(LAS u32x4*)p = v; }
DEV void lw8(LAS char* p, u32x2 v) { *(LAS u32x2*)p = v; }
DEV u32x4 cat(u32x2 a, u32x2 b) { return u32x4{a.x, a.y, b.x, b.y}; }
DEV u32x8 cat(u32x4 a, u32x4 b) { return u32x8{a.x, a.y, a.z, a.w, b.x, b.y, b.z, b.w}; }
DEV f32x4 mma(u32x4 a, u32x4 b, f32x4 c) {
  return __builtin_amdgcn_mfma_f32_16x16x32_bf16(__builtin_bit_cast(bfv8, a), __builtin_bit_cast(bfv8, b), c, 0, 0, 0);
}
DEV f32x4 mma16(u32x2 a, u32x2 b, f32x4 c) {
  return __builtin_amdgcn_mfma_f32_16x16x16bf16_1k(__builtin_bit_cast(s16x4, a), __builtin_bit_cast(s16x4, b), c, 0, 0, 0);
}
DEV f32x4 smm(u32x4 a, u32x8 b, f32x4 c, u32 idx) {
  return __builtin_amdgcn_smfmac_f32_16x16x64_bf16(__builtin_bit_cast(bfv8, a), __builtin_bit_cast(bf16v, b), c, idx, 0, 0);
}
#define MEMB asm volatile("" ::: "memory")
DEV float max3(float a, float b, float c) { float r; asm("v_max3_f32 %0, %1, %2, %3" : "=v"(r) : "v"(a), "v"(b), "v"(c)); return r; }
DEV void bar() { asm volatile("s_waitcnt lgkmcnt(0)\n\ts_barrier" ::: "memory"); }
template <int N> DEV void waitvm() { asm volatile("s_waitcnt vmcnt(%0)" ::"i"(N) : "memory"); }
DEV void waitlds() { asm volatile("s_waitcnt lgkmcnt(0)" ::: "memory"); }
DEV void dma16(const void* g, u32 ldsaddr) {
  u32 keep;
  asm volatile("s_mov_b32 %0, m0\n\ts_mov_b32 m0, %2\n\ts_nop 0\n\tglobal_load_lds_dwordx4 %1, off\n\ts_mov_b32 m0, %0"
               : "=&s"(keep) : "v"(g), "s"(ldsaddr) : "memory");
}
DEV float sw16(float a, bool mx) {
  auto r = __builtin_amdgcn_permlane16_swap(__float_as_uint(a), __float_as_uint(a), false, false);
  float x = __uint_as_float(r[0]), y = __uint_as_float(r[1]);
  return mx ? fmaxf(x, y) : x + y;
}
DEV float sw32(float a, bool mx) {
  auto r = __builtin_amdgcn_permlane32_swap(__float_as_uint(a), __float_as_uint(a), false, false);
  float x = __uint_as_float(r[0]), y = __uint_as_float(r[1]);
  return mx ? fmaxf(x, y) : x + y;
}
template <int C> DEV float ror_add(float u) {
  return u + __int_as_float(__builtin_amdgcn_update_dpp(0, __float_as_int(u), 0x120 + C, 0xf, 0xf, true));
}
DEV f32x4 fold(f32x4 x, f32x4 y) {
  f32x4 o;
#pragma unroll
  for (int i = 0; i < 4; ++i) {
    auto r = __builtin_amdgcn_permlane32_swap(__float_as_uint(x[i]), __float_as_uint(y[i]), false, false);
    o[i] = __uint_as_float(r[0]) + __uint_as_float(r[1]);
  }
  return o;
}
DEV f32x4 vzero() { f32x4 z; asm volatile("v_mov_b32 %0, 0" : "=v"(z[0])); asm volatile("v_mov_b32 %0, 0" : "=v"(z[1])); asm volatile("v_mov_b32 %0, 0" : "=v"(z[2])); asm volatile("v_mov_b32 %0, 0" : "=v"(z[3])); return z; }
DEV u32x2 pk4(f32x4 v) { return u32x2{pk(v[0], v[1]), pk(v[2], v[3])}; }
DEV u32 nosent(u32 v) { return v < 0xfffffffeu ? v : 0xfffffffeu; }
DEV bool valid(u32x4 a) { return a.x != ~0u && a.y != ~0u && a.z != ~0u && a.w != ~0u; }
DEV u32 nearoff(u32 off, u32 side) { return ((off >> 13) << 14) | (off & 8191) | (side << 13); }
DEV u32 memoff(u32 k, u32 quad) {
  u32 hb = (k >> 4) & 1;
  return (k >> 6) * 1024 + hb * 512 + quad * 256 + ((k & 15) ^ (8 * quad)) * 16 + (((k >> 5) & 1) ^ hb) * 8;
}
template <int N> DEV void delay() {
#pragma unroll
  for (int i = 0; i < N; ++i) asm volatile("s_nop 7" ::: "memory");
}
DEV u32 umax3(u32 a, u32 b, u32 c) { u32 r; asm("v_max3_u32 %0, %1, %2, %3" : "=v"(r) : "v"(a), "v"(b), "v"(c)); return r; }
DEV float fadd(float a, float b) { float r; asm("v_add_f32 %0, %1, %2" : "=v"(r) : "v"(a), "v"(b)); return r; }
DEV float fmul(float a, float b) { float r; asm("v_mul_f32 %0, %1, %2" : "=v"(r) : "v"(a), "v"(b)); return r; }
DEV f32x4 add4(f32x4 a, f32x4 b) { return f32x4{fadd(a[0], b[0]), fadd(a[1], b[1]), fadd(a[2], b[2]), fadd(a[3], b[3])}; }
DEV f32x4 mul4(f32x4 a, float b) { return f32x4{fmul(a[0], b), fmul(a[1], b), fmul(a[2], b), fmul(a[3], b)}; }
DEV void halt() { asm volatile("s_endpgm" ::: "memory"); __builtin_unreachable(); }
DEV bool nosentinel(u32 m) { return __builtin_amdgcn_ballot_w64(m == ~0u) == 0; }
#define SLP 8
DEV u32 wait_flags(rsrc_t rw, u32 flag, u32 target, bool active, bool fast = false) {
  for (u32 spins = 0;; ++spins) {
    asm volatile("" ::: "memory");
    u32 v = ld4(rw, O_CTRL + (active ? flag : F_TMO) * 128, 0);
    if (__all(!active || v >= target)) return __builtin_amdgcn_readfirstlane(v);
    if (spins > SPIN_CAP || __any(!active && v != 0)) { st4(1, rw, O_CTRL + F_TMO * 128, 0); return 0; }
    if (fast) __builtin_amdgcn_s_sleep(1); else __builtin_amdgcn_s_sleep(SLP);
  }
}
#define STAMP(i)
#define PROFV(i, v)
#define CYC(i)
DEV void add_flag(char* ws, u32 flag) {
  __hip_atomic_fetch_add((__attribute__((address_space(1))) u32*)(ws + O_CTRL + flag * 128), 1u, __ATOMIC_RELAXED, __HIP_MEMORY_SCOPE_AGENT);
}

#define NPROBE 3
__global__ __launch_bounds__(256) void prologue_kernel(Params p) {
  __shared__ float red[4][32];
  const int tid = threadIdx.x, b = blockIdx.x, l = tid & 63, w = tid >> 6;
  if (b == 0) STAMP(0);
  rsrc_t rw = mkrsrc(align_ws(p.wsraw), WS_END);
  for (u32 j = b * 256 + tid; j < 2 * MB / 16; j += 65536) {
    u32x4 v = {~0u, ~0u, ~0u, ~0u};
    if (j < 1024) {
      u32 ch = j & 511, hb = (ch >> 5) & 1, quad = (ch >> 4) & 1, kk = (ch >> 6) * 64 + hb * 16 + ((ch & 15) ^ (8 * quad));
      const unsigned short* m = (const unsigned short*)p.mem0 + quad * 4 * 512;
      u32 ka = kk + hb * 32, kb = kk + (hb ^ 1) * 32;
      v = u32x4{nosent(m[ka] | (u32)m[512 + ka] << 16), nosent(m[1024 + ka] | (u32)m[1536 + ka] << 16),
                nosent(m[kb] | (u32)m[512 + kb] << 16), nosent(m[1024 + kb] | (u32)m[1536 + kb] << 16)};
    }
    st16(v, rw, j * 16, 0);
  }
  if (b >= 96 && b < 99) { u32 f = (b - 96) * 256 + tid; if (f < F_END) st4(0, rw, O_CTRL + f * 128, 0); }
  if (b < 96) {
    const int kq = tid >> 2, cq = tid & 3;
    u32x4 cv = *(const u32x4*)(p.c + kq * 8);
    float acc[8] = {0, 0, 0, 0, 0, 0, 0, 0};
#pragma unroll
    for (int i = 0; i < 8; ++i) {
      float cc = (i & 1) ? hi(cv[i >> 1]) : lo(cv[i >> 1]);
      float s = rb(cc / (1.f + __expf(-cc)));
      u32x4 wv = *(const u32x4*)(p.wmod + (size_t)(kq * 8 + i) * 3072 + b * 32 + cq * 8);
#pragma unroll
      for (int j = 0; j < 4; ++j) { acc[2 * j] += s * lo(wv[j]); acc[2 * j + 1] += s * hi(wv[j]); }
    }
#pragma unroll
    for (int j = 0; j < 8; ++j) { float t = sw32(sw16(ror_add<8>(ror_add<4>(acc[j])), false), false); if (l < 4) red[w][l * 8 + j] = t; }
    __syncthreads();
    if (tid < 32) ((float*)(align_ws(p.wsraw) + O_MOD))[b * 32 + tid] = rb(red[0][tid] + red[1][tid] + red[2][tid] + red[3][tid]);
  }
}

constexpr u32 L_MEM = 131072, L_K = L_MEM + 8192, L_Q = L_K + 8192, L_KM = L_Q + 2048, L_O = L_KM + 2048, L_V = L_O + 4096, L_END = L_V + 8192;
#define D1 6
#define D2 19

DEV void stage_qkv(const Params& p, int i, int h, int w, int l, u32 smb) {
  const __bf16* W = i < 4 ? p.wq : (i < 8 ? p.wv : p.wk);
#pragma unroll
  for (int j = 0; j < 8; ++j) {
    const int row = w * 32 + j * 4 + (l >> 4), sw = 2 * ((l >> 4) | ((j >> 1) & 1) << 2);
    dma16((const char*)(W + (size_t)((i & 3) * 128 + row) * 4096 + h * 128) + ((l & 15) ^ sw) * 16, smb + (i & 3) * 32768 + w * 8192 + j * 1024);
  }
}
DEV void read_qkv(LAS char* src, int w, int l, u32x8 (&W)[8][2], int B0) {
  const int g = l >> 4, q4 = (l >> 2) & 3, p4 = l & 3, sw = 2 * (q4 | (g & 1) << 2);
#pragma unroll
  for (int t = 0; t < 2; ++t) {
    LAS char* a = src + (g * 8 + q4) * 256 + ((w * 4 + t * 2 + (p4 >> 1)) ^ sw) * 16 + (p4 & 1) * 8;
#pragma unroll
    for (int B = 0; B < 2; ++B) {
      LAS char* q = a + B * 16384;
      W[B0 + B][t] = cat(cat(trd(q), trd(q + 1024)), cat(trd(q + 8192), trd(q + 9216)));
    }
  }
}

DEV void role_recur(const Params& p, LAS char* sm, int r) {
  const int tid = threadIdx.x, l = tid & 63, w = __builtin_amdgcn_readfirstlane(tid >> 6);
  const int h = (r & 3) + 4 * (r >> 3), c = (r >> 2) & 1, rr = h * 2 + c;
  const int g = l >> 4, n = l & 15, q4 = (l >> 2) & 3, p4 = l & 3;
  rsrc_t rw = mkrsrc(align_ws(p.wsraw), WS_END);
  if (r == 0) {
    const char* q = align_ws(p.wsraw) + O_MEM + 4096 + w * 1024;
    u32 best[2] = {~0u, ~0u}, d;
    asm volatile("global_load_dword %0, %1, off sc1\n\ts_waitcnt vmcnt(0)" : "=v"(d) : "v"(q - 4096) : "memory");
#pragma unroll 1
    for (int i = 0; i < NPROBE; ++i)
#pragma unroll
      for (int e = 0; e < 2; ++e) {
        unsigned long long t1, t2;
        asm volatile("s_memtime %0\n\ts_waitcnt lgkmcnt(0)\n\tglobal_load_dword %2, %3, off sc1\n\ts_waitcnt vmcnt(0)\n\ts_memtime %1\n\ts_waitcnt lgkmcnt(0)"
                     : "=&s"(t1), "=&s"(t2), "=&v"(d) : "v"(q + e * 8192 + i * 256) : "memory");
        best[e] = min(best[e], (u32)(t2 - t1));
      }
    LAS u32* lat = (LAS u32*)(sm + L_MEM);
    lat[w * 2] = best[0]; lat[w * 2 + 1] = best[1];
    bar();
    const u32 l0 = min(min(lat[0], lat[2]), min(lat[4], lat[6])), l1 = min(min(lat[1], lat[3]), min(lat[5], lat[7]));
    if (tid == 0) st4(l0 <= l1 ? 1u : 2u, rw, O_CTRL + F_SIDE * 128, 0);
    PROFV(11, l0 * 10000 + l1);
    STAMP(12);
  }
  const u32 smb = (u32)(size_t)sm;
  LAS char* stA = sm + w * 32768; LAS char* stB = stA + 16384;
  if (r == 0) STAMP(1);
  u32 np1 = 0, np2 = 0;
  u32x8 Wq[8][2], Wv[8][2], Wk[8][2];
  stage_qkv(p, 0, h, w, l, smb); stage_qkv(p, 1, h, w, l, smb); stage_qkv(p, 2, h, w, l, smb);
#pragma unroll
  for (int i = 0; i < 12; ++i) {
    if (i == 10) waitvm<8>(); else waitvm<16>();
    bar();
    if (i < 9) stage_qkv(p, i + 3, h, w, l, smb);
    if (i == 10) {
      const char* gp = (const char*)(p.wo + (size_t)(h * 128 + (l >> 3)) * 512 + c * 256 + w * 64) + (l & 7) * 16;
#pragma unroll
      for (int j = 0; j < 16; ++j) dma16(gp + j * 8 * 1024, smb + w * 16384 + j * 1024);
    }
    read_qkv(sm + (i & 3) * 32768, w, l, i < 4 ? Wq : (i < 8 ? Wv : Wk), (i & 3) * 2);
    waitlds();
  }
  {
    u32x8 T[4][2];
    waitvm<0>();
    LAS char* a = sm + w * 16384 + (16 * (g & 1) + 4 * (g >> 1) + (q4 & 1)) * 128 + (q4 >> 1) * 32 + p4 * 8;
#pragma unroll
    for (int eb = 0; eb < 4; ++eb)
#pragma unroll
      for (int t = 0; t < 2; ++t) {
        LAS char* q = a + eb * 4096 + t * 64;
        T[eb][t] = cat(cat(trd(q), trd(q + 256)), cat(trd(q + 1024), trd(q + 1280)));
      }
    bar();
#pragma unroll
    for (int B = 0; B < 4; ++B)
#pragma unroll
      for (int t = 0; t < 2; ++t) {
        const u32x8 x = T[B][t], y = Wk[4 + B][t];
        lw16(stA + (B * 4 + t * 2) * 1024 + l * 16, u32x4{x[0], x[1], x[2], x[3]});
        lw16(stA + (B * 4 + t * 2 + 1) * 1024 + l * 16, u32x4{x[4], x[5], x[6], x[7]});
        lw16(stB + (B * 4 + t * 2) * 1024 + l * 16, u32x4{y[0], y[1], y[2], y[3]});
        lw16(stB + (B * 4 + t * 2 + 1) * 1024 + l * 16, u32x4{y[4], y[5], y[6], y[7]});
      }
  }
  u32 side = wait_flags(rw, F_SIDE, 1, l == 0);
  if (!side) halt();
  side = (side - 1) & 1;
  const u32 idx = (l & 8) ? 0xEEEEu : 0x4444u;
  u32x4 sel;
#pragma unroll
  for (int j = 0; j < 4; ++j) sel[j] = ((l & 7) == 2 * j ? 0x3f80u : 0u) | ((l & 7) == 2 * j + 1 ? 0x3f800000u : 0u);
  LAS char* aA0 = sm + L_MEM + memoff(16 * g + 4 * (q4 >> 1) + (q4 & 1) + 2 * (p4 >> 1), p4 & 1);
  LAS char* aA1 = sm + L_MEM + memoff(16 * g + 8 + 4 * (q4 >> 1) + (q4 & 1) + 2 * (p4 >> 1), p4 & 1);
  LAS char* aXw = sm + L_Q + w * 512 + (g & 1) * 256 + ((16 * (g >> 1) + n) ^ (4 * (g & 1))) * 8;
  LAS char* aX0 = sm + L_Q + (p4 & 1) * 256 + ((8 * g + q4) ^ (4 * (p4 & 1))) * 8;
  LAS char* aX1 = sm + L_Q + (p4 & 1) * 256 + ((8 * g + 4 + q4) ^ (4 * (p4 & 1))) * 8;
  const u32 vMem = side * 8192 + w * 2048 + l * 16;
  const u32 vPst = nearoff((c * 32 + w * 8 + (l >> 3)) * 4096 + h * 128 + (l & 7) * 16, side);
  const u32 vPld = nearoff(rr * 4096 + (l >> 1) * 128 + w * 32 + (l & 1) * 16, side);
  u32 vMst;
  {
    u32 ch = rr * 8 + w * 2 + (l & 1), gg = (ch >> 4) & 3;
    vMst = side * 8192 + memoff((ch >> 6) * 64 + 32 * (g & 1) + 16 * (gg >> 1) + (ch & 15), gg & 1);
  }
  const u32 vKV = h * 8192 + w * 2048 + l * 16, vKV2 = vKV + 1024;
  if (r == 0) STAMP(2);
  STAMP(600 + r);
  u32 prog = wait_flags(rw, F_KV + h, 1, l == 0);
  if (!prog) halt();
  STAMP(664 + r);
  u32x4 kn0 = ld16(rw, vKV, O_K), kn1 = ld16(rw, vKV + 1024, O_K), vn0 = ld16(rw, vKV, O_V), vn1 = ld16(rw, vKV + 1024, O_V);
  const f32x4 z4 = {0.f, 0.f, 0.f, 0.f};
  const u32x4 ones = {0x3f803f80u, 0x3f803f80u, 0x3f803f80u, 0x3f803f80u};
  const u32x2 ones2 = {0x3f803f80u, 0x3f803f80u};
  const float sc = 0.08838834764831845f * 1.4426950408889634f;

  LAS char* aWo = stA + l * 16; LAS char* aWk = stB + l * 16;
  LAS char* aK = sm + L_K + l * 16; LAS char* aO = sm + L_O + l * 16; LAS char* aM = sm + L_MEM + w * 2048 + l * 16;
  LAS char* aV = sm + L_V + w * 2048 + l * 16;
#define PIN(x) asm volatile("" : "+v"(x))
#define PIN2(x, y) asm volatile("" : "+v"(x), "+v"(y))
#define NOPD(n, x, y) asm volatile("s_nop " #n : "+v"(x), "+v"(y))
#define SMA(d, a, b) asm volatile("v_smfmac_f32_16x16x64_bf16 %0, %1, %2, %3" : "+v"(d) : "v"(a), "a"(b), "v"(idx))
#define SMV(d, a, b) asm volatile("v_smfmac_f32_16x16x64_bf16 %0, %1, %2, %3" : "+v"(d) : "v"(a), "v"(b), "v"(idx))
#define MM0(d, a, b) asm volatile("v_mfma_f32_16x16x32_bf16 %0, %1, %2, 0" : "=&v"(d) : "v"(a), "v"(b))
#define MMA(d, a, b) asm volatile("v_mfma_f32_16x16x32_bf16 %0, %1, %2, %0" : "+v"(d) : "v"(a), "v"(b))
#define M16(d, a, b) asm volatile("v_mfma_f32_16x16x16_bf16 %0, %1, %2, %0" : "+v"(d) : "v"(a), "v"(b))
#define SWP(x, y, i) do { auto r_ = __builtin_amdgcn_permlane32_swap(__float_as_uint(x[i]), __float_as_uint(y[i]), false, false); \
    x[i] = __uint_as_float(r_[0]); y[i] = __uint_as_float(r_[1]); } while (0)
#define LW(a, i) cat(lr16((a) + (i) * 2048), lr16((a) + (i) * 2048 + 1024))
#define PUBLISH(s) do { if (c == 0) { const u32x4 kmw = cat(trd(aX0 + 2048 + w * 512), trd(aX1 + 2048 + w * 512)); \
      if (!(l & 8)) st16(kmw, rw, h * 2048 + w * 512 + g * 128 + (l & 7) * 16, O_KM + (s) * 65536); \
    } else st8(vm, rw, h * 2048 + w * 512 + l * 8, O_VM + (s) * 65536); } while (0)
#define LDA(d, v, so) asm volatile("buffer_load_dwordx4 %0, %1, %2, %3 offen sc1" : "=&v"(d) : "v"(v), "s"(rw), "s"(so) : "memory")
#define EX(x) __builtin_amdgcn_exp2f(__builtin_fmaf(x, sc, mx))
  lw16(aK + w * 2048, kn0); lw16(aK + w * 2048 + 1024, kn1);
  lw16(aV, vn0); lw16(aV + 1024, vn1);
  u32x2 vm = {0u, 0u};
  const u32 vPub = h * 2048 + w * 512 + (c ? l * 8 : g * 128 + (l & 7) * 16);
  for (int s = 0;; ++s) {
    f32x4 qx = z4, qy = z4, kx = z4, ky = z4;
    PIN2(qx, qy); PIN2(kx, ky);
    Wk[4][0] = LW(aWk, 0); Wk[4][1] = LW(aWk, 1); Wk[5][0] = LW(aWk, 2); Wk[5][1] = LW(aWk, 3);
    delay<D2>();
    CYC(0);
    bool ok;
    u32 spins = 0;
#pragma unroll 1
    do {
      MEMB;
      const u32x4 ma = ld16(rw, vMem, O_MEM + s * 16384), mb = ld16(rw, vMem + 1024, O_MEM + s * 16384);
      np2 += s >= 20;
      lw16(aM, ma); lw16(aM + 1024, mb);
      ok = nosentinel(umax3(umax3(ma.x, ma.y, ma.z), umax3(mb.x, mb.y, mb.z), umax3(ma.w, mb.w, mb.w)));
    } while (!ok && ++spins < SPIN_CAP);
    if (!ok) { st4(2, rw, O_CTRL + F_TMO * 128, 0); halt(); }
    CYC(1);
    bar();
    CYC(2);
    u32x4 af[8];
#define AF(B) do { MEMB; af[B] = cat(trd(aA0 + (B) * 1024), trd(aA1 + (B) * 1024)); MEMB; } while (0)
    AF(0); AF(1); AF(2); AF(3);
    SMA(qx, af[0], Wq[0][0]); AF(4); SMA(qy, af[0], Wq[0][1]); AF(5);
    SMA(qx, af[1], Wq[1][0]); AF(6); SMA(qy, af[1], Wq[1][1]); AF(7);
    const u32x4 kmp = cat(trd(aX0 + 2048 + w * 512), trd(aX1 + 2048 + w * 512)); MEMB;
    SMA(qx, af[2], Wq[2][0]); MEMB;
    const u32 s1 = s < 63 ? s + 1 : 63;
    if (prog < s1 + 1) {
      prog = wait_flags(rw, F_KV + (s1 >> 5) * 48 + h, (s1 & 31) + 1, l == 0);
      if (!prog) halt();
      prog += s1 & 32;
    }
    LDA(kn0, vKV, O_K + s1 * 262144);
    SMA(qy, af[2], Wq[2][1]);
#pragma unroll
    for (int B = 3; B < 8; ++B) {
      SMA(qx, af[B], Wq[B][0]);
      if (B == 3 && s) { if (c) st8(vm, rw, vPub, O_VM + (s - 1) * 65536); }
      if (B == 4) LDA(kn1, vKV2, O_K + s1 * 262144);
      if (B == 5 && s) { if (!c && !(l & 8)) st16(kmp, rw, vPub, O_KM + (s - 1) * 65536); }
      if (B == 7 && s) { if (tid == 0) st4(s - 1, rw, O_CTRL + (F_KM + rr) * 128, 0); }
      if (B == 6) LDA(vn0, vKV, O_V + s1 * 262144);
      SMA(qy, af[B], Wq[B][1]);
      if (B < 7) { MEMB; Wk[6 + ((B - 3) >> 1)][(B - 3) & 1] = LW(aWk, B + 1); MEMB; }
    }
#pragma unroll
    for (int B = 4; B < 7; ++B) {
      SMV(kx, af[B], Wk[B][0]);
      if (B == 4) LDA(vn1, vKV2, O_V + s1 * 262144);
      SMV(ky, af[B], Wk[B][1]);
    }
    NOPD(1, qx, qy);
    SWP(qx, qy, 0); SWP(qx, qy, 1); PIN2(qx, qy);
    SMV(kx, af[7], Wk[7][0]);
    SWP(qx, qy, 2); SWP(qx, qy, 3); PIN2(qx, qy);
    SMV(ky, af[7], Wk[7][1]);
    qx = add4(qx, qy); PIN(qx);
    SMV(kx, af[0], Wk[0][0]);
    { u32x2 t2 = pk4(qx); PIN(t2); SMV(ky, af[0], Wk[0][1]); MEMB; lw8(aXw, t2); MEMB; }
    u32x4 qf[4], kf[4][2], km[4];
    f32x4 s0, s1v, sma, vx = z4, vy = z4;
    PIN2(vx, vy);
    kf[0][0] = lr16(aK); kf[0][1] = lr16(aK + 1024); MEMB;
    SMV(kx, af[1], Wk[1][0]); MEMB;
    SMV(ky, af[1], Wk[1][1]); MEMB;
    kf[1][0] = lr16(aK + 2048); kf[1][1] = lr16(aK + 3072); MEMB;
    SMV(kx, af[2], Wk[2][0]); MEMB;
    kf[2][0] = lr16(aK + 4096); kf[2][1] = lr16(aK + 5120); MEMB;
    SMV(ky, af[2], Wk[2][1]); MEMB;
    kf[3][0] = lr16(aK + 6144); kf[3][1] = lr16(aK + 7168); MEMB;
    SMV(kx, af[3], Wk[3][0]); SMV(ky, af[3], Wk[3][1]);
    SMA(vx, af[0], Wv[0][0]); SMA(vy, af[0], Wv[0][1]);
    NOPD(0, kx, ky);
    SWP(kx, ky, 0); SWP(kx, ky, 1); PIN2(kx, ky);
    SMA(vx, af[1], Wv[1][0]);
    SWP(kx, ky, 2); SWP(kx, ky, 3); PIN2(kx, ky);
    SMA(vy, af[1], Wv[1][1]);
    kx = add4(kx, ky);
    { u32x2 t2 = pk4(kx); PIN(t2); SMA(vx, af[2], Wv[2][0]); MEMB; lw8(aXw + 2048, t2); MEMB; }
    SMA(vy, af[2], Wv[2][1]); MEMB;
    SMA(vx, af[3], Wv[3][0]); SMA(vy, af[3], Wv[3][1]);
    bar();
    CYC(3);
    qf[0] = cat(trd(aX0), trd(aX1)); km[0] = cat(trd(aX0 + 2048), trd(aX1 + 2048)); MEMB;
    SMA(vx, af[4], Wv[4][0]); MEMB;
    qf[1] = cat(trd(aX0 + 512), trd(aX1 + 512)); km[1] = cat(trd(aX0 + 2560), trd(aX1 + 2560)); MEMB;
    SMA(vy, af[4], Wv[4][1]); MEMB;
    qf[2] = cat(trd(aX0 + 1024), trd(aX1 + 1024)); km[2] = cat(trd(aX0 + 3072), trd(aX1 + 3072)); MEMB;
    SMA(vx, af[5], Wv[5][0]); MEMB;
    qf[3] = cat(trd(aX0 + 1536), trd(aX1 + 1536)); km[3] = cat(trd(aX0 + 3584), trd(aX1 + 3584)); MEMB;
    SMA(vy, af[5], Wv[5][1]); MEMB;
    const u32x4 vc0 = lr16(aV), vc1 = lr16(aV + 1024); MEMB;
    SMA(vx, af[6], Wv[6][0]); SMA(vy, af[6], Wv[6][1]);
    MM0(s0, kf[0][0], qf[0]); MM0(s1v, kf[0][1], qf[0]); MM0(sma, km[0], qf[0]);
#pragma unroll
    for (int eb = 1; eb < 4; ++eb) { MMA(s0, kf[eb][0], qf[eb]); MMA(s1v, kf[eb][1], qf[eb]); MMA(sma, km[eb], qf[eb]); }
    SMA(vx, af[7], Wv[7][0]); MEMB;
    u32x8 Wo[4][2];
    Wo[0][0] = LW(aWo, 0); Wo[0][1] = LW(aWo, 1); MEMB;
    PIN2(s0, s1v);
    float mx = max3(max3(s0[0], s0[1], s0[2]), max3(s0[3], s1v[0], s1v[1]), max3(s1v[2], s1v[3], s1v[3]));
    PIN(mx);
    SMA(vy, af[7], Wv[7][1]); MEMB;
    Wo[1][0] = LW(aWo, 2); Wo[1][1] = LW(aWo, 3); MEMB;
    PIN(sma);
    mx = max3(max3(mx, sma[0], sma[1]), sma[2], sma[3]);
    { auto r_ = __builtin_amdgcn_permlane16_swap(__float_as_uint(mx), __float_as_uint(mx), false, false); mx = max3(__uint_as_float(r_[0]), __uint_as_float(r_[1]), __uint_as_float(r_[1])); }
    { auto r_ = __builtin_amdgcn_permlane32_swap(__float_as_uint(mx), __float_as_uint(mx), false, false); mx = max3(__uint_as_float(r_[0]), __uint_as_float(r_[1]), __uint_as_float(r_[1])) * -sc; }
    asm volatile("" : "+v"(mx), "+v"(vx), "+v"(vy));
#pragma unroll
    for (int i = 0; i < 4; ++i) {
      s0[i] = EX(s0[i]); s1v[i] = EX(s1v[i]);
      SWP(vx, vy, i); vx[i] = fadd(vx[i], vy[i]);
    }
    u32x4 pf = cat(pk4(s0), pk4(s1v));
    vm = pk4(vx);
    asm volatile("s_nop 0" : "+v"(pf), "+v"(vm));
    f32x4 ox, oy, su;
    MM0(su, ones, pf);
    sma[0] = EX(sma[0]); sma[1] = EX(sma[1]); PIN(sma);
    MM0(ox, vc0, pf);
    sma[2] = EX(sma[2]); sma[3] = EX(sma[3]); PIN(sma);
    MM0(oy, vc1, pf);
    u32x2 pm = pk4(sma), pmx, pmy;
    pmx.x = g < 2 ? pm.x : 0u; pmx.y = g < 2 ? pm.y : 0u; pmy.x = g < 2 ? 0u : pm.x; pmy.y = g < 2 ? 0u : pm.y;
    asm volatile("s_nop 0" : "+v"(pmx), "+v"(pmy));
    M16(su, ones2, pmx); M16(ox, vm, pmx); M16(oy, vm, pmy); MEMB;
    Wo[2][0] = LW(aWo, 4); Wo[2][1] = LW(aWo, 5); Wo[3][0] = LW(aWo, 6); Wo[3][1] = LW(aWo, 7);
    asm volatile("" : "+v"(su) :: "memory");
    float inv = __builtin_amdgcn_rcpf(su[0]);
    PIN(inv); PIN2(ox, oy);
    CYC(4);
    if (s == 63) { asm volatile("s_waitcnt vmcnt(0)" :: "v"(kn0), "v"(kn1), "v"(vn0), "v"(vn1) : "memory"); PUBLISH(63); break; }
    {
      lw16(aO + w * 1024, cat(pk4(mul4(ox, inv)), pk4(mul4(oy, inv)))); MEMB;
      f32x4 d0 = z4, d1 = z4;
      PIN2(d0, d1);
      if (s >= 61) waitvm<0>();
      bar();
      CYC(5);
      u32x4 of[4];
#pragma unroll
      for (int eb = 0; eb < 4; ++eb) of[eb] = lr16(aO + eb * 1024);
      MEMB;
#pragma unroll
      for (int eb = 0; eb < 4; ++eb) { SMV(d0, of[eb], Wo[eb][0]); SMV(d1, of[eb], Wo[eb][1]); }
      asm volatile("s_nop 6" : "+v"(d0));
      u32x2 o0 = pk4(d0); PIN(o0); PIN(d1);
      u32x2 o1 = pk4(d1);
      st16(u32x4{nosent(o0.x), nosent(o0.y), nosent(o1.x), nosent(o1.y)}, rw, vPst, O_RING + (s & 1) * 524288);
      MEMB;
      CYC(6);
      asm volatile("s_waitcnt vmcnt(1)\n\tds_write_b128 %4, %0\n\tds_write_b128 %4, %1 offset:1024\n\tds_write_b128 %5, %2\n\tds_write_b128 %5, %3 offset:1024"
                   :: "v"(kn0), "v"(kn1), "v"(vn0), "v"(vn1), "v"(aK + w * 2048), "v"(aV) : "memory");
    }
    if (s >= 61 && tid == 0) st4(s, rw, O_CTRL + (F_KM + rr) * 128, 0);
    if (r == 0) STAMP(16 + s);
    if (s < 3) STAMP(728 + s * 64 + r);
    if (s < 62 && prog < s + 3) {
      const u32 v = __builtin_amdgcn_readfirstlane(ld4(rw, O_CTRL + (F_KV + ((s + 2) >> 5) * 48 + h) * 128, 0));
      if (v && prog >= ((s + 2) & 32)) prog = v + ((s + 2) & 32);
    } else delay<D1>();
    CYC(7);
    f32x4 t;
    spins = 0;
#pragma unroll 1
    do {
      MEMB;
      const u32x4 pp = ld16(rw, vPld, O_RING + (s & 1) * 524288);
      np1 += s >= 20;
      MM0(t, sel, pp);
      ok = nosentinel(umax3(umax3(pp.x, pp.y, pp.z), pp.w, pp.w));
    } while (!ok && ++spins < SPIN_CAP);
    if (!ok) { st4(3, rw, O_CTRL + F_TMO * 128, 0); halt(); }
    CYC(8);
#define RA(i, n) "v_add_f32_dpp %" #i ", %" #i ", %" #i " row_ror:" #n " row_mask:0xf bank_mask:0xf\n\t"
    asm volatile("s_nop 3\n\t" RA(0, 2) RA(1, 2) RA(2, 2) RA(3, 2) RA(0, 4) RA(1, 4) RA(2, 4) RA(3, 4) RA(0, 8) RA(1, 8) RA(2, 8) RA(3, 8)
                 : "+v"(t[0]), "+v"(t[1]), "+v"(t[2]), "+v"(t[3]));
    u32x2 mo = pk4(t);
    if ((l & 0x2e) == 0) st8(u32x2{nosent(mo.x), nosent(mo.y)}, rw, vMst, O_MEM + (s + 1) * 16384);
    MEMB;
    CYC(9);
    st16(u32x4{~0u, ~0u, ~0u, ~0u}, rw, vPld, O_RING + (s & 1) * 524288);
  }
  waitvm<0>();
  bar();
  if (r == 0) { STAMP(3); STAMP(79); PROFV(8, np1); PROFV(9, np2); PROFV(10, side); }
  if (tid == 0) st4(64, rw, O_CTRL + (F_KM + rr) * 128, 0);
}

template <int ROWS, int RB> DEV void stage_w(const char* g, size_t rowstride, int l, u32 dst) {
  constexpr int LPR = RB / 16, RPI = 64 / LPR;
  const char* a = g + (size_t)(l / LPR) * rowstride + (l % LPR) * 16;
#pragma unroll
  for (int i = 0; i < ROWS / RPI; ++i) dma16(a + (size_t)i * RPI * rowstride, dst + i * 1024);
}
DEV bool wg_wait(rsrc_t rw, LAS char* sm, int w, u32 flag, u32 target, bool active) {
  LAS u32* okw = (LAS u32*)(sm + 155584);
  if (w == 0) *okw = wait_flags(rw, flag, target, active);
  bar();
  const u32 ok = *okw;
  bar();
  return ok != 0;
}

DEV void phase_ln1(const Params& p, rsrc_t rw, LAS char* sm, int wk) {
  const int tid = threadIdx.x, l = tid & 63, w = tid >> 6;
  const float* mod = (const float*)(align_ws(p.wsraw) + O_MOD);
  f32x4 sa0 = *(const f32x4*)(mod + l * 8), sa1 = *(const f32x4*)(mod + l * 8 + 4);
  f32x4 ca0 = *(const f32x4*)(mod + 512 + l * 8), ca1 = *(const f32x4*)(mod + 512 + l * 8 + 4);
  u32x4 xv[4];
#pragma unroll
  for (int i = 0; i < 4; ++i) xv[i] = *(const u32x4*)(p.x + (size_t)(wk * 16 + w * 4 + i) * 512 + l * 8);
#pragma unroll
  for (int i = 0; i < 4; ++i) {
    float v[8], s = 0.f, q = 0.f;
#pragma unroll
    for (int j = 0; j < 4; ++j) { v[2 * j] = lo(xv[i][j]); v[2 * j + 1] = hi(xv[i][j]); s += v[2 * j] + v[2 * j + 1]; }
    s = sw32(sw16(ror_add<8>(ror_add<4>(ror_add<2>(ror_add<1>(s)))), false), false) * (1.f / 512);
#pragma unroll
    for (int j = 0; j < 8; ++j) { v[j] -= s; q += v[j] * v[j]; }
    q = sw32(sw16(ror_add<8>(ror_add<4>(ror_add<2>(ror_add<1>(q)))), false), false) * (1.f / 512);
    const float rs = rsqrtf(q + 1e-5f);
    u32x4 o;
#pragma unroll
    for (int j = 0; j < 4; ++j) {
      const float c0 = j < 2 ? ca0[2 * j] : ca1[2 * j - 4], c1 = j < 2 ? ca0[2 * j + 1] : ca1[2 * j - 3];
      const float a0 = j < 2 ? sa0[2 * j] : sa1[2 * j - 4], a1 = j < 2 ? sa0[2 * j + 1] : sa1[2 * j - 3];
      o[j] = pk(v[2 * j] * rs * (1.f + c0) + a0, v[2 * j + 1] * rs * (1.f + c1) + a1);
    }
    lw16(sm + 131072 + l * 256 + (((w * 4 + i) ^ l) & 15) * 16, o);
  }
  bar();
#pragma unroll
  for (int i = 0; i < 4; ++i) {
    const int ch = i * 256 + tid;
    st16(lr16(sm + 131072 + (ch >> 4) * 256 + ((ch ^ (ch >> 4)) & 15) * 16), rw, wk * 16384 + ch * 16, O_H);
  }
  waitvm<0>();
  bar();
  if (tid == 0) add_flag(align_ws(p.wsraw), F_H + (wk >> 1));
}

template <bool ISV> DEV bool qkv_loop(rsrc_t rw, LAS char* sm, const u32x4 (&W)[16][4], int rg, u32 obase, u32 flag) {
  const int tid = threadIdx.x, l = tid & 63, w = tid >> 6;
  const f32x4 z4 = {0.f, 0.f, 0.f, 0.f};
  u32x4 pf[8];
#pragma unroll
  for (int j = 0; j < 8; ++j) pf[j] = ld16(rw, w * 8192 + j * 1024 + l * 16, O_H + rg * 32 * 32768);
#pragma unroll
  for (int j = 0; j < 8; ++j) lw16(sm + w * 8192 + j * 1024 + l * 16, pf[j]);
  bar();
  for (int i = 0; i < 32; ++i) {
    const int seg = rg * 32 + i;
    if (i < 31) {
#pragma unroll
      for (int j = 0; j < 8; ++j) pf[j] = ld16(rw, w * 8192 + j * 1024 + l * 16, O_H + (seg + 1) * 32768);
    }
    LAS char* a = sm + (i & 1) * 32768 + l * 16;
    f32x4 acc[2][4];
#pragma unroll
    for (int t = 0; t < 4; ++t) { acc[0][t] = z4; acc[1][t] = z4; }
#pragma unroll
    for (int ks = 0; ks < 16; ++ks) {
      const u32x4 h0 = lr16(a + ks * 1024), h1 = lr16(a + 16384 + ks * 1024);
#pragma unroll
      for (int t = 0; t < 4; ++t) {
        acc[0][t] = ISV ? mma(h0, W[ks][t], acc[0][t]) : mma(W[ks][t], h0, acc[0][t]);
        acc[1][t] = ISV ? mma(h1, W[ks][t], acc[1][t]) : mma(W[ks][t], h1, acc[1][t]);
      }
    }
#pragma unroll
    for (int t = 0; t < 4; ++t) {
      const u32x4 o = ISV ? cat(pk4(acc[0][t]), pk4(acc[1][t])) : cat(pk4(acc[t & 1][t & 2]), pk4(acc[t & 1][(t & 2) + 1]));
      st16(o, rw, t * 1024 + l * 16, obase + seg * 262144);
    }
    if (i < 3 || i == 31) waitvm<0>(); else waitvm<4>();
    if (i < 31) {
#pragma unroll
      for (int j = 0; j < 8; ++j) lw16(sm + ((i + 1) & 1) * 32768 + w * 8192 + j * 1024 + l * 16, pf[j]);
    }
    bar();
    if (tid == 0) st4(i < 3 || i == 31 ? i + 1 : i, rw, O_CTRL + flag * 128, 0);
  }
  return true;
}

#define PREF 1
#define RG1_AFTER 1
DEV bool phase_qkv(const Params& p, rsrc_t rw, LAS char* sm, int wk) {
  const int tid = threadIdx.x, l = tid & 63, w = __builtin_amdgcn_readfirstlane(tid >> 6);
  const int cb = wk % 48, rg = wk / 48, g = l >> 4, q4 = (l >> 2) & 3, p4 = l & 3;
  const bool isv = cb < 32 && w >= 2;
  const int head = cb < 32 ? cb : 2 * (cb - 32) + (w >> 1);
  const __bf16* M = cb >= 32 ? p.wq : (isv ? p.wv : p.wk);
  const char* gsrc = (const char*)(M + head * 128 + (w & 1) * 64);
  const u32 dst = (u32)(size_t)sm + w * 32768;
  if (rg && wk < 176) phase_ln1(p, rw, sm, wk - 48);
  if (wk >= 128 && PREF) {
    if (!wg_wait(rw, sm, w, F_SIDE, 1, l == 0)) return false;
    const int i = (wk - 128) >> 2, hh = (wk & 3) + 4 * (i >> 1), hf = i & 1;
    const size_t o = (size_t)(hf * 256 + w * 64 + (l >> 4)) * 8192 + hh * 256 + (l & 15) * 16;
    const char* go = (const char*)p.wo + (size_t)(hh * 128 + w * 32 + (l >> 5)) * 1024 + hf * 512 + (l & 31) * 16;
#pragma unroll 4
    for (int j = 0; j < 16; ++j) dma16((const char*)p.wv + o + (size_t)j * 32768, dst + j * 1024);
#pragma unroll 4
    for (int j = 0; j < 16; ++j) dma16((const char*)p.wk + o + (size_t)j * 32768, dst + j * 1024);
#pragma unroll 4
    for (int j = 0; j < 16; ++j) dma16(go + j * 2048, dst + j * 1024);
    waitvm<0>();
  }
  if (rg) {
    if (rg > 1) return true;
    if (!wg_wait(rw, sm, w, F_KV + cb, RG1_AFTER, l == 0)) return false;
  }
  stage_w<128, 128>(gsrc, 8192, l, dst); stage_w<128, 128>(gsrc + 128 * 8192, 8192, l, dst + 16384);
  u32x4 W[16][4];
  LAS char* a = sm + w * 32768 + (8 * g + q4) * 128 + (isv ? p4 * 8 : p4 * 16);
  const int tstep = isv ? 32 : 8;
#pragma unroll
  for (int c = 0; c < 4; ++c) {
    if (c < 3) waitvm<16>(); else waitvm<0>();
#pragma unroll
    for (int k = 0; k < 4; ++k)
#pragma unroll
      for (int t = 0; t < 4; ++t) {
        LAS char* q = a + (c & 1) * 16384 + k * 4096 + (t >> 1) * 64 + (t & 1) * tstep;
        W[c * 4 + k][t] = cat(trd(q), trd(q + 512));
      }
    waitlds();
    if (c < 2) stage_w<128, 128>(gsrc + (size_t)(c + 2) * 128 * 8192, 8192, l, dst + (c & 1) * 16384);
  }
  if (wk == 0) STAMP(4);
  if (!wg_wait(rw, sm, w, F_H + rg * 32 + l, 2, l < 32)) return false;
  if (wk == 0) STAMP(5);
  const u32 obase = (cb >= 32 ? O_Q : (isv ? O_V : O_K)) + head * 8192 + (w & 1) * 4096;
  if (isv) qkv_loop<true>(rw, sm, W, rg, obase, F_KV + rg * 48 + cb);
  else qkv_loop<false>(rw, sm, W, rg, obase, F_KV + rg * 48 + cb);
  return true;
}

DEV void role_attn(const Params& p, rsrc_t rw, LAS char* sm, int set, int hp) {
  const int tid = threadIdx.x, l = tid & 63, w = __builtin_amdgcn_readfirstlane(tid >> 6);
  const int g = l >> 4, n = l & 15, q4 = (l >> 2) & 3, p4 = l & 3;
  const f32x4 z4 = {0.f, 0.f, 0.f, 0.f};
  const char* gsrc = (const char*)(p.wo + (size_t)hp * 256 * 512 + w * 128);
  const u32 dst = (u32)(size_t)sm + w * 32768;
  u32x4 W[8][8];
  stage_w<64, 256>(gsrc, 1024, l, dst); stage_w<64, 256>(gsrc + 64 * 1024, 1024, l, dst + 16384);
  LAS char* a = sm + w * 32768 + (4 * g + q4) * 256 + p4 * 16;
#pragma unroll
  for (int c = 0; c < 4; ++c) {
    if (c < 3) waitvm<16>(); else waitvm<0>();
#pragma unroll
    for (int k = 0; k < 2; ++k)
#pragma unroll
      for (int t = 0; t < 8; ++t) {
        LAS char* q = a + (c & 1) * 16384 + k * 8192 + (t >> 1) * 64 + (t & 1) * 8;
        W[c * 2 + k][t] = cat(trd(q), trd(q + 4096));
      }
    waitlds();
    if (c < 2) stage_w<64, 256>(gsrc + (c + 2) * 64 * 1024, 1024, l, dst + (c & 1) * 16384);
  }
  const int hd = hp * 2 + (w >> 1), qt = w & 1;
  const float sc = 0.08838834764831845f * 1.4426950408889634f;
  bar();
  for (int s = set; s < 64; s += 3) {
    {
      const u32 rgb = F_KV + (s >> 5) * 48;
      if (!wait_flags(rw, l < 1 ? rgb + hd : rgb + 32 + hp, (s & 31) + 1, l < 2)) halt();
    }
    const u32 vo = hd * 8192 + l * 16, so = s * 262144;
    u32x4 kf[4][2], qf[4], km[4], vf[4][2]; u32x2 vm[4];
#pragma unroll
    for (int eb = 0; eb < 4; ++eb) {
      qf[eb] = ld16(rw, vo + eb * 2048 + qt * 1024, O_Q + so);
      kf[eb][0] = ld16(rw, vo + eb * 2048, O_K + so); kf[eb][1] = ld16(rw, vo + eb * 2048 + 1024, O_K + so);
    }
#pragma unroll
    for (int eb = 0; eb < 4; ++eb) { vf[eb][0] = ld16(rw, vo + eb * 2048, O_V + so); vf[eb][1] = ld16(rw, vo + eb * 2048 + 1024, O_V + so); }
    f32x4 s0 = z4, s1 = z4, sm_ = z4;
#pragma unroll
    for (int eb = 0; eb < 4; ++eb) { s0 = mma(kf[eb][0], qf[eb], s0); s1 = mma(kf[eb][1], qf[eb], s1); }
    MEMB;
    if (!wait_flags(rw, F_KM + hd * 2 + l, s + 1, l < 2, s > 61)) halt();
    if (s == 63 && hp == 0) STAMP(930);
#pragma unroll
    for (int eb = 0; eb < 4; ++eb) km[eb] = ld16(rw, hd * 2048 + eb * 512 + g * 128 + (l & 7) * 16, O_KM + s * 65536);
#pragma unroll
    for (int eb = 0; eb < 4; ++eb) vm[eb] = ld8(rw, hd * 2048 + eb * 512 + l * 8, O_VM + s * 65536);
#pragma unroll
    for (int eb = 0; eb < 4; ++eb) sm_ = mma(km[eb], qf[eb], sm_);
    float mx = -3.0e38f;
#pragma unroll
    for (int i = 0; i < 4; ++i) {
      s0[i] = (4 * g + i <= 16 * qt + n) ? s0[i] * sc : -1.4426950408889634e9f;
      s1[i] = (16 + 4 * g + i <= 16 * qt + n) ? s1[i] * sc : -1.4426950408889634e9f;
      sm_[i] *= sc;
      mx = fmaxf(mx, fmaxf(fmaxf(s0[i], s1[i]), sm_[i]));
    }
    mx = sw32(sw16(mx, true), true);
    float sum = 0.f, summ = 0.f;
#pragma unroll
    for (int i = 0; i < 4; ++i) {
      s0[i] = __builtin_amdgcn_exp2f(s0[i] - mx); s1[i] = __builtin_amdgcn_exp2f(s1[i] - mx); sm_[i] = __builtin_amdgcn_exp2f(sm_[i] - mx);
      sum += s0[i] + s1[i]; summ += sm_[i];
    }
    const float inv = __builtin_amdgcn_rcpf(sw32(sw16(sum + 0.5f * summ, false), false));
    const u32x4 pf = cat(pk4(s0), pk4(s1));
    const u32x2 pm = pk4(sm_), zz = {0u, 0u}, pmx = g < 2 ? pm : zz, pmy = g < 2 ? zz : pm;
#pragma unroll
    for (int eb = 0; eb < 4; ++eb) {
      const f32x4 ox = mma16(vm[eb], pmx, mma(vf[eb][0], pf, z4)), oy = mma16(vm[eb], pmy, mma(vf[eb][1], pf, z4));
      lw16(sm + (qt * 8 + (w >> 1) * 4 + eb) * 1024 + l * 16, cat(pk4(ox * inv), pk4(oy * inv)));
    }
    bar();
#pragma unroll
    for (int t2 = 0; t2 < 2; ++t2) {
      f32x4 acc[8];
#pragma unroll
      for (int t = 0; t < 8; ++t) acc[t] = z4;
#pragma unroll
      for (int ks = 0; ks < 8; ++ks) {
        const u32x4 of = lr16(sm + (t2 * 8 + ks) * 1024 + l * 16);
#pragma unroll
        for (int t = 0; t < 8; ++t) acc[t] = mma(W[ks][t], of, acc[t]);
      }
#pragma unroll
      for (int u = 0; u < 4; ++u)
        st16(cat(pk4(acc[2 * u]), pk4(acc[2 * u + 1])), rw, hp * 32768 + t2 * 16384 + (w * 4 + u) * 1024 + l * 16, O_P1 + s * 524288);
    }
    waitvm<0>();
    bar();
    if (tid == 0) add_flag(align_ws(p.wsraw), F_P1 + s);
    if (s == 63 && hp == 0) STAMP(931);
  }
}

DEV void role_norm(const Params& p, rsrc_t rw, LAS char* sm, int set, int j) {
  const int tid = threadIdx.x, l = tid & 63, w = __builtin_amdgcn_readfirstlane(tid >> 6), t4 = tid & 3, pc = tid >> 2;
  const float* mod = (const float*)(align_ws(p.wsraw) + O_MOD) + pc * 8;
  float ga[8], sb[8], cb[8];
#pragma unroll
  for (int i = 0; i < 8; ++i) { ga[i] = mod[1024 + i]; sb[i] = mod[1536 + i]; cb[i] = 1.f + mod[2048 + i]; }
  LAS float* red = (LAS float*)sm;
  const u32 vo = (j >> 2) * 16384 + pc * 256 + ((4 * j + t4) & 15) * 16;
  for (int s = set; s < 64; s += 3) {
    const u32x4 xv = *(const u32x4*)(p.x + (size_t)(s * 32 + 4 * j + t4) * 512 + pc * 8);
    if (!wait_flags(rw, F_P1 + s, 16, l < 1, s > 61)) halt();
    if (s == 63 && j == 0) STAMP(932);
    u32x4 pp[16];
#pragma unroll
    for (int i = 0; i < 16; ++i) pp[i] = ld16(rw, vo + i * 32768, O_P1 + s * 524288);
    float v[8] = {0, 0, 0, 0, 0, 0, 0, 0};
#pragma unroll
    for (int i = 0; i < 16; ++i)
#pragma unroll
      for (int e = 0; e < 4; ++e) { v[2 * e] += lo(pp[i][e]); v[2 * e + 1] += hi(pp[i][e]); }
    u32x4 x1; float su = 0.f, q = 0.f;
#pragma unroll
    for (int e = 0; e < 4; ++e) {
      x1[e] = pk(lo(xv[e]) + ga[2 * e] * rb(v[2 * e]), hi(xv[e]) + ga[2 * e + 1] * rb(v[2 * e + 1]));
      v[2 * e] = lo(x1[e]); v[2 * e + 1] = hi(x1[e]); su += v[2 * e] + v[2 * e + 1];
    }
    st16(x1, rw, vo, O_X1 + s * 32768);
    su = sw32(sw16(ror_add<8>(ror_add<4>(su)), false), false);
    if (l < 4) red[w * 4 + l] = su;
    bar();
    su = (red[t4] + red[4 + t4] + red[8 + t4] + red[12 + t4]) * (1.f / 512);
#pragma unroll
    for (int e = 0; e < 8; ++e) { v[e] -= su; q += v[e] * v[e]; }
    q = sw32(sw16(ror_add<8>(ror_add<4>(q)), false), false);
    if (l < 4) red[16 + w * 4 + l] = q;
    bar();
    const float rs = rsqrtf((red[16 + t4] + red[20 + t4] + red[24 + t4] + red[28 + t4]) * (1.f / 512) + 1e-5f);
    u32x4 o;
#pragma unroll
    for (int e = 0; e < 4; ++e) o[e] = pk(v[2 * e] * rs * cb[2 * e] + sb[2 * e], v[2 * e + 1] * rs * cb[2 * e + 1] + sb[2 * e + 1]);
    st16(o, rw, vo, O_H2 + s * 32768);
    waitvm<0>();
    bar();
    if (tid == 0) add_flag(align_ws(p.wsraw), F_H2 + s);
    if (s == 63 && j == 0) STAMP(933);
  }
}

DEV float gelu(float x) {
  const float u = x + 0.044715f * x * x * x;
  return x * __builtin_amdgcn_rcpf(1.f + __builtin_amdgcn_exp2f(u * (-2.f * 0.7978845608028654f * 1.4426950408889634f)));
}
DEV void role_mlp(const Params& p, rsrc_t rw, LAS char* sm, int set, int fs) {
  const int tid = threadIdx.x, l = tid & 63, w = __builtin_amdgcn_readfirstlane(tid >> 6);
  const int g = l >> 4, q4 = (l >> 2) & 3, p4 = l & 3;
  const f32x4 z4 = {0.f, 0.f, 0.f, 0.f};
  const bool pad = fs == 10 && w == 3;
  const int f0 = pad ? 0 : fs * 128 + w * 32;
  const u32 dst = (u32)(size_t)sm + w * 32768;
  u32x4 W1[16][2], W2[4][8];
  const char* g1 = (const char*)(p.w1 + f0);
  const char* g2 = (const char*)(p.w2 + (size_t)fs * 128 * 512 + w * 128);
  stage_w<256, 64>(g1, 2752, l, dst); stage_w<256, 64>(g1 + 256 * 2752, 2752, l, dst + 16384);
#pragma unroll
  for (int c = 0; c < 2; ++c) {
    waitvm<16>();
    LAS char* a = sm + w * 32768 + c * 16384 + (8 * g + q4) * 64 + p4 * 16;
#pragma unroll
    for (int k = 0; k < 8; ++k)
#pragma unroll
      for (int t = 0; t < 2; ++t) W1[c * 8 + k][t] = cat(trd(a + k * 2048 + t * 8), trd(a + k * 2048 + t * 8 + 256));
    waitlds();
    stage_w<32, 256>(g2 + c * 64 * 1024, 1024, l, dst + c * 16384);
    stage_w<32, 256>(g2 + ((c == 1 && fs == 10) ? 0 : (c * 64 + 32) * 1024), 1024, l, dst + c * 16384 + 8192);
  }
#pragma unroll
  for (int c = 0; c < 2; ++c) {
    if (c == 0) waitvm<16>(); else waitvm<0>();
    LAS char* a = sm + w * 32768 + c * 16384 + (8 * g + q4) * 256 + p4 * 16;
#pragma unroll
    for (int k = 0; k < 2; ++k)
#pragma unroll
      for (int t = 0; t < 8; ++t) {
        LAS char* q = a + k * 8192 + (t >> 1) * 64 + (t & 1) * 8;
        W2[c * 2 + k][t] = cat(trd(q), trd(q + 1024));
      }
  }
  if (fs == 10) {
#pragma unroll
    for (int t = 0; t < 8; ++t) W2[3][t] = u32x4{0u, 0u, 0u, 0u};
  }
  const u32x4 b1v = *(const u32x4*)(p.b1 + f0 + g * 8);
  bar();
  for (int s = set; s < 64; s += 3) {
    if (!wait_flags(rw, F_H2 + s, 8, l < 1, s > 61)) halt();
    if (s == 63 && fs == 0) STAMP(934);
    u32x4 pf[8];
#pragma unroll
    for (int j = 0; j < 8; ++j) pf[j] = ld16(rw, w * 8192 + j * 1024 + l * 16, O_H2 + s * 32768);
#pragma unroll
    for (int j = 0; j < 8; ++j) lw16(sm + w * 8192 + j * 1024 + l * 16, pf[j]);
    bar();
#pragma unroll
    for (int t2 = 0; t2 < 2; ++t2) {
      f32x4 a0 = {lo(b1v[0]), hi(b1v[0]), lo(b1v[1]), hi(b1v[1])}, a1 = {lo(b1v[2]), hi(b1v[2]), lo(b1v[3]), hi(b1v[3])};
#pragma unroll
      for (int ks = 0; ks < 16; ++ks) {
        const u32x4 hf = lr16(sm + t2 * 16384 + ks * 1024 + l * 16);
        a0 = mma(W1[ks][0], hf, a0); a1 = mma(W1[ks][1], hf, a1);
      }
#pragma unroll
      for (int i = 0; i < 4; ++i) { a0[i] = pad ? 0.f : gelu(rb(a0[i])); a1[i] = pad ? 0.f : gelu(rb(a1[i])); }
      lw16(sm + 32768 + (t2 * 4 + w) * 1024 + l * 16, cat(pk4(a0), pk4(a1)));
    }
    bar();
#pragma unroll
    for (int t2 = 0; t2 < 2; ++t2) {
      f32x4 acc[8];
#pragma unroll
      for (int t = 0; t < 8; ++t) acc[t] = z4;
#pragma unroll
      for (int ks = 0; ks < 4; ++ks) {
        const u32x4 gf = lr16(sm + 32768 + (t2 * 4 + ks) * 1024 + l * 16);
#pragma unroll
        for (int t = 0; t < 8; ++t) acc[t] = mma(W2[ks][t], gf, acc[t]);
      }
#pragma unroll
      for (int u = 0; u < 4; ++u)
        st16(cat(pk4(acc[2 * u]), pk4(acc[2 * u + 1])), rw, fs * 32768 + t2 * 16384 + (w * 4 + u) * 1024 + l * 16, O_P2 + s * 360448);
    }
    waitvm<0>();
    bar();
    if (tid == 0) add_flag(align_ws(p.wsraw), F_P2 + s);
    if (s == 63 && fs == 0) STAMP(935);
  }
}

DEV void role_out(const Params& p, rsrc_t rw, LAS char* sm, int set, int j) {
  const int tid = threadIdx.x, l = tid & 63, w = __builtin_amdgcn_readfirstlane(tid >> 6), t4 = tid & 3, pc = tid >> 2;
  const float* mod = (const float*)(align_ws(p.wsraw) + O_MOD) + pc * 8;
  float gb[8];
#pragma unroll
  for (int i = 0; i < 8; ++i) gb[i] = mod[2560 + i];
  const u32x4 b2v = *(const u32x4*)(p.b2 + pc * 8);
  const u32 vo = (j >> 2) * 16384 + pc * 256 + ((4 * j + t4) & 15) * 16;
  for (int s = set; s < 64; s += 3) {
    if (!wait_flags(rw, F_P2 + s, 11, l < 1, s > 61)) halt();
    if (s == 63 && j == 0) STAMP(936);
    u32x4 pp[11];
#pragma unroll
    for (int i = 0; i < 11; ++i) pp[i] = ld16(rw, vo + i * 32768, O_P2 + s * 360448);
    const u32x4 x1 = ld16(rw, vo, O_X1 + s * 32768);
    u32x4 o;
#pragma unroll
    for (int e = 0; e < 4; ++e) {
      float a = 0.f, b = 0.f;
#pragma unroll
      for (int i = 0; i < 11; ++i) { a += lo(pp[i][e]); b += hi(pp[i][e]); }
      o[e] = pk(lo(x1[e]) + gb[2 * e] * rb(rb(a) + lo(b2v[e])), hi(x1[e]) + gb[2 * e + 1] * rb(rb(b) + hi(b2v[e])));
    }
    *(u32x4*)(p.out + (size_t)(s * 32 + 4 * j + t4) * 512 + pc * 8) = o;
  }
}

__global__ __launch_bounds__(256, 1) void main_kernel(Params p) {
  __shared__ __attribute__((aligned(16))) char smem[L_END];
  LAS char* sm = (LAS char*)smem;
  const int b = blockIdx.x, i4 = (b >> 3) * 4 + (b & 3);
  if ((b & 4) == 0 && b < 128) { role_recur(p, sm, i4); return; }
  const int wk = (b & 4) ? i4 : 64 + i4;
  rsrc_t rw = mkrsrc(align_ws(p.wsraw), WS_END);
  if (!phase_qkv(p, rw, sm, wk)) return;
  STAMP(128 + wk);
  if (wk < 48) role_attn(p, rw, sm, wk / 16, wk % 16);
  else if (wk < 81) role_mlp(p, rw, sm, (wk - 48) / 11, (wk - 48) % 11);
  else if (wk < 105) role_norm(p, rw, sm, (wk - 81) / 8, (wk - 81) % 8);
  else if (wk >= 128 && wk < 152) role_out(p, rw, sm, (wk - 128) / 8, (wk - 128) % 8);
  STAMP(384 + wk);
}

extern "C" void kernel_launch(void* const* d_in, const int* in_sizes, int n_in, void* d_out, int out_size, void* d_ws,
                              size_t ws_size, hipStream_t stream) {
  Params p;
  p.x = (const __bf16*)d_in[0]; p.mem0 = (const __bf16*)d_in[1]; p.wq = (const __bf16*)d_in[2]; p.wk = (const __bf16*)d_in[3];
  p.wv = (const __bf16*)d_in[4]; p.wo = (const __bf16*)d_in[5]; p.w1 = (const __bf16*)d_in[8]; p.b1 = (const __bf16*)d_in[9];
  p.w2 = (const __bf16*)d_in[10]; p.b2 = (const __bf16*)d_in[11]; p.c = (const __bf16*)d_in[12]; p.wmod = (const __bf16*)d_in[13];
  p.out = (__bf16*)d_out;
  p.wsraw = (char*)d_ws;
  prologue_kernel<<<256, 256, 0, stream>>>(p);
  main_kernel<<<256, 256, 0, stream>>>(p);
}
```
